```python
import torch

def _softmax(x, axis=-1):
    return torch.softmax(x, dim=axis)
def _gelu(x):
    return torch.nn.functional.gelu(x, approximate="tanh")
def _silu(x):
    return torch.nn.functional.silu(x)
def _sigmoid(x):
    return torch.sigmoid(x)
def _layernorm(x, axis=-1, eps=1e-5):
    m = x.mean(dim=axis, keepdim=True)
    v = x.var(dim=axis, keepdim=True, unbiased=False)
    return (x - m) / torch.sqrt(v + eps)
def _scatter_add(tgt, idx, src, axis=0):
    return tgt.clone().index_add_(axis, idx, src)
def _segment_sum(src, seg, n):
    out = torch.zeros((n,) + src.shape[1:], dtype=src.dtype)
    return out.index_add_(0, seg, src)
def _pad(x, pw):
    flat = tuple(v for pair in reversed(pw) for v in pair)
    return torch.nn.functional.pad(x, flat)

kernel_name = 'standalone__molecular_angular_gemnet__graph'

def setup_inputs():

    _w = (lambda a: (lambda t: t.to(torch.bfloat16) if t.is_floating_point() else t)(torch.as_tensor(a)))
    N, deg, De, L = 10000, 32, 16, 1
    E = N * deg
    T = E * deg
    NR = 8
    dist = torch.rand(E).to(float)
    angle = (torch.rand(T) * torch.pi).to(float)
    centers = torch.linspace(0.0, 1.0, NR).to(float)
    ang_freq = torch.arange(1, L + 1).to(float)
    return {
        "m": _w((lambda *s: torch.randn(*s, dtype=torch.bfloat16) * 0.05)(E, De)),
        "idx_in": _w((lambda lo, hi, *s: torch.randint(lo, hi, s))(0, E, T).to(torch.int64)),
        "idx_out": _w((lambda lo, hi, *s: torch.randint(lo, hi, s))(0, E, T).to(torch.int64)),
        "dist": _w(dist),
        "angle": _w(angle),
        "centers": _w(centers),
        "ang_freq": _w(ang_freq),
        "w_rbf": _w((lambda *s: torch.randn(*s, dtype=torch.bfloat16) * 0.2)(NR, De)),
        "w_sbf": _w((lambda *s: torch.randn(*s, dtype=torch.bfloat16) * 0.2)(NR * L, De)),
        "w_down": _w((lambda *s: torch.randn(*s, dtype=torch.bfloat16) * 0.2)(De, De)),
        "w_up": _w((lambda *s: torch.randn(*s, dtype=torch.bfloat16) * 0.2)(De, De)),
    }

def reference(m, idx_in, idx_out, dist, angle, centers, ang_freq, w_rbf, w_sbf, w_down, w_up):
    E, De = m.shape[0], m.shape[1]
    NR = centers.shape[0]
    L = ang_freq.shape[0]
    rbf = torch.exp(-10.0 * (dist[:, None] - centers[None, :]) ** 2)
    cbf = torch.cos(ang_freq[None, :] * angle[:, None])
    rbf_t = rbf[idx_out]
    sbf = torch.reshape(rbf_t[:, :, None] * cbf[:, None, :], (-1, NR * L))
    x = (m[idx_in] @ w_down) * (sbf @ w_sbf)
    agg = _segment_sum(x, idx_out, E)
    return m + (agg @ w_up) * (rbf @ w_rbf)
```

```cpp
#include <hip/hip_runtime.h>
#include <hip/hip_bf16.h>
#include <stdint.h>
#include <stdio.h>

typedef unsigned short u16;
__device__ __forceinline__ float bf2f(u16 v) { return __uint_as_float(((unsigned)v) << 16); }
__device__ __forceinline__ float rbf16(float x) { return __bfloat162float(__float2bfloat16(x)); }

__global__ void k_zero(float4* S, int n4) {
    int i = blockIdx.x * blockDim.x + threadIdx.x;
    if (i < n4) S[i] = make_float4(0, 0, 0, 0);
}

__global__ void k_scatter(const u16* __restrict__ m, const int* __restrict__ idx_in, const int* __restrict__ idx_out,
                          const u16* __restrict__ angle, const u16* __restrict__ ang_freq, float* S, int T) {
    int g = blockIdx.x * blockDim.x + threadIdx.x;
    int t = g >> 4, f = g & 15;
    if (t >= T) return;
    float fr = bf2f(ang_freq[0]);
    float c = rbf16(cosf(rbf16(fr * bf2f(angle[t]))));
    int ii = idx_in[t], io = idx_out[t];
    float v = bf2f(m[(size_t)ii * 16 + f]) * c;
    atomicAdd(&S[(size_t)io * 16 + f], v);
}

__global__ void __launch_bounds__(256) k_epi(const u16* __restrict__ m, const u16* __restrict__ dist, const u16* __restrict__ centers,
                      const u16* __restrict__ w_rbf, const u16* __restrict__ w_sbf, const u16* __restrict__ w_down,
                      const u16* __restrict__ w_up, const float* __restrict__ S, __hip_bfloat16* out, int E) {
    int e = blockIdx.x * blockDim.x + threadIdx.x;
    if (e >= E) return;
    float rbf[8];
    float d = bf2f(dist[e]);
    _Pragma("unroll") for (int r = 0; r < 8; r++) {
        float x = rbf16(d - bf2f(centers[r]));
        x = rbf16(x * x);
        x = rbf16(-10.0f * x);
        rbf[r] = rbf16(expf(x));
    }
    float agg[16];
    _Pragma("unroll") for (int j = 0; j < 16; j++) {
        float a = 0, g = 0;
        _Pragma("unroll") for (int k = 0; k < 16; k++) a += S[(size_t)e * 16 + k] * bf2f(w_down[k * 16 + j]);
        _Pragma("unroll") for (int r = 0; r < 8; r++) g += rbf[r] * bf2f(w_sbf[r * 16 + j]);
        agg[j] = a * g;
    }
    _Pragma("unroll") for (int j = 0; j < 16; j++) {
        float a = 0, g = 0;
        _Pragma("unroll") for (int k = 0; k < 16; k++) a += agg[k] * bf2f(w_up[k * 16 + j]);
        _Pragma("unroll") for (int r = 0; r < 8; r++) g += rbf[r] * bf2f(w_rbf[r * 16 + j]);
        out[(size_t)e * 16 + j] = __float2bfloat16(bf2f(m[(size_t)e * 16 + j]) + a * g);
    }
}

extern "C" void kernel_launch(void* const* d_in, const int* in_sizes, int n_in,
                              void* d_out, int out_size, void* d_ws, size_t ws_size,
                              hipStream_t stream) {
    const u16* m = (const u16*)d_in[0];
    const int* idx_in = (const int*)d_in[1];
    const int* idx_out = (const int*)d_in[2];
    const u16* dist = (const u16*)d_in[3];
    const u16* angle = (const u16*)d_in[4];
    const u16* centers = (const u16*)d_in[5];
    const u16* ang_freq = (const u16*)d_in[6];
    const u16* w_rbf = (const u16*)d_in[7];
    const u16* w_sbf = (const u16*)d_in[8];
    const u16* w_down = (const u16*)d_in[9];
    const u16* w_up = (const u16*)d_in[10];
    int E = in_sizes[3], T = in_sizes[1];
    static int once = 0;
    if (!once) { once = 1; printf("n_in %d sizes:", n_in); for (int i = 0; i < n_in; i++) printf(" %d", in_sizes[i]); printf(" out %d ws %zu\n", out_size, ws_size);
      for (int i = 0; i < n_in; i++) printf(" %p", d_in[i]); printf(" out %p ws %p\n", d_out, d_ws); }
    float* S = (float*)d_ws;
    int n4 = E * 4;
    k_zero<<<(n4 + 255) / 256, 256, 0, stream>>>((float4*)S, n4);
    long long tot = (long long)T * 16;
    k_scatter<<<(int)((tot + 255) / 256), 256, 0, stream>>>(m, idx_in, idx_out, angle, ang_freq, S, T);
    k_epi<<<(E + 255) / 256, 256, 0, stream>>>(m, dist, centers, w_rbf, w_sbf, w_down, w_up, S, (__hip_bfloat16*)d_out, E);
}
```

```cpp
#include <hip/hip_runtime.h>
#include <hip/hip_bf16.h>
#include <stdint.h>

typedef unsigned short u16;
typedef unsigned u32;
typedef unsigned long long u64;
typedef u32 u32x2 __attribute__((ext_vector_type(2)));
typedef u32 u32x3 __attribute__((ext_vector_type(3)));
typedef u32 u32x4 __attribute__((ext_vector_type(4)));
typedef float f32x4 __attribute__((ext_vector_type(4)));
typedef float f32x8 __attribute__((ext_vector_type(8)));
typedef __bf16 bf16x2_t __attribute__((ext_vector_type(2)));
typedef __bf16 bf16x8_t __attribute__((ext_vector_type(8)));

#define P_NPH 5
#define P_CAP 8448
#define P_IN_AUX 2
#define P_RECST_AUX 0
#define P_RECLD_AUX 0
#define P_WS_OFF 0

namespace {

constexpr int E_ = 320000;
constexpr int T_ = 10240000;
constexpr int NOB = 256;
constexpr int EPB = E_ / NOB;
constexpr int NPH = P_NPH;
constexpr int IBS = (E_ + NPH - 1) / NPH;
constexpr int NC = 20480;
constexpr int NCH = T_ / NC;
constexpr int NBIN = NPH * NOB;
constexpr int RPT = NC / 1024;
constexpr int NG = RPT / 4;
static_assert(NCH * NC == T_, "chunking");
static_assert(NOB * EPB == E_, "blocks");
static_assert(NBIN % 64 == 0 && (NBIN / 64) % 4 == 0, "scan layout");

constexpr int ctz_(int d) { return (d & 1) ? 0 : 1 + ctz_(d >> 1); }
template <int D> struct Magic {
    static constexpr bool direct = ((((1ull << 32) + D - 1) / D) * D - (1ull << 32)) * (u64)E_ < (1ull << 32);
    static constexpr int SH = direct ? 0 : ctz_(D);
    static constexpr int DD = D >> SH;
    static constexpr u32 M = (u32)(((1ull << 32) + DD - 1) / DD);
    static_assert(M < (1u << 24), "magic too large");
    static_assert(((u64)M * DD - (1ull << 32)) * (u64)((E_ >> SH) + 1) < (1ull << 32), "magic inexact");
};
__device__ __forceinline__ u32 mulhi24(u32 a, u32 b) {
    u32 r; asm("v_mul_hi_u32_u24 %0, %1, %2" : "=v"(r) : "s"(b), "v"(a)); return r;
}
template <int D> __device__ __forceinline__ u32 divc(u32 x) { return mulhi24(x >> Magic<D>::SH, Magic<D>::M); }

__device__ __forceinline__ float bf2f(u32 v16) { return __uint_as_float(v16 << 16); }
__device__ __forceinline__ u32 pkbf(float lo, float hi) {
    bf16x2_t v = {(__bf16)lo, (__bf16)hi};
    return __builtin_bit_cast(u32, v);
}
__device__ __forceinline__ float rbf16(float x) { return __uint_as_float(pkbf(x, 0.f) << 16); }

__device__ const u32 kInv2PiFrac[125] = {
    0x028BE60Eu, 0x0517CC1Bu, 0x0A2F9837u, 0x145F306Eu, 0x28BE60DCu, 0x517CC1B7u, 0xA2F9836Eu, 0x45F306DDu,
    0x8BE60DB9u, 0x17CC1B72u, 0x2F9836E5u, 0x5F306DCAu, 0xBE60DB94u, 0x7CC1B727u, 0xF9836E4Eu, 0xF306DC9Du,
    0xE60DB939u, 0xCC1B7272u, 0x9836E4E4u, 0x306DC9C9u, 0x60DB9391u, 0xC1B72722u, 0x836E4E44u, 0x06DC9C88u,
    0x0DB93910u, 0x1B727221u, 0x36E4E441u, 0x6DC9C883u, 0xDB939105u, 0xB727220Bu, 0x6E4E4415u, 0xDC9C882Au,
    0xB9391055u, 0x727220A9u, 0xE4E44153u, 0xC9C882A5u, 0x9391054Au, 0x27220A95u, 0x4E44152Au, 0x9C882A54u,
    0x391054A8u, 0x7220A950u, 0xE44152A0u, 0xC882A540u, 0x91054A7Fu, 0x220A94FEu, 0x441529FCu, 0x882A53F8u,
    0x1054A7F1u, 0x20A94FE1u, 0x41529FC2u, 0x82A53F85u, 0x054A7F0Au, 0x0A94FE14u, 0x1529FC27u, 0x2A53F84Fu,
    0x54A7F09Du, 0xA94FE13Bu, 0x529FC275u, 0xA53F84EBu, 0x4A7F09D6u, 0x94FE13ACu, 0x29FC2758u, 0x53F84EB0u,
    0xA7F09D5Fu, 0x4FE13ABFu, 0x9FC2757Du, 0x3F84EAFAu, 0x7F09D5F4u, 0xFE13ABE9u, 0xFC2757D2u, 0xF84EAFA4u,
    0xF09D5F48u, 0xE13ABE90u, 0xC2757D1Fu, 0x84EAFA3Fu, 0x09D5F47Du, 0x13ABE8FBu, 0x2757D1F5u, 0x4EAFA3EAu,
    0x9D5F47D5u, 0x3ABE8FAAu, 0x757D1F53u, 0xEAFA3EA7u, 0xD5F47D4Du, 0xABE8FA9Au, 0x57D1F535u, 0xAFA3EA6Au,
    0x5F47D4D3u, 0xBE8FA9A7u, 0x7D1F534Eu, 0xFA3EA69Cu, 0xF47D4D37u, 0xE8FA9A6Fu, 0xD1F534DEu, 0xA3EA69BCu,
    0x47D4D377u, 0x8FA9A6EEu, 0x1F534DDCu, 0x3EA69BB8u, 0x7D4D3770u, 0xFA9A6EE0u, 0xF534DDC1u, 0xEA69BB82u,
    0xD4D37703u, 0xA9A6EE07u, 0x534DDC0Eu, 0xA69BB81Bu, 0x4D377037u, 0x9A6EE06Eu, 0x34DDC0DBu, 0x69BB81B7u,
    0xD377036Eu, 0xA6EE06DBu, 0x4DDC0DB6u, 0x9BB81B6Cu, 0x377036D9u, 0x6EE06DB1u, 0xDDC0DB63u, 0xBB81B6C5u,
    0x77036D8Au, 0xEE06DB15u, 0xDC0DB629u, 0xB81B6C53u, 0x7036D8A5u};

__device__ __forceinline__ float cos_small(float x) { return __builtin_amdgcn_cosf(x * 0.15915494309189535f); }
__device__ __forceinline__ float cos_any(float x) {
    const u32 b = (__float_as_uint(x) >> 16) & 0x7fffu;
    const u32 eb = b >> 7;
    const u32 ti = min(max(eb, 130u), 254u) - 130u;
    const u32 mm = (b & 0x7fu) | 0x80u;
    const u32 rev = mm * kInv2PiFrac[ti];
    float arg = (float)rev * 2.3283064365386963e-10f;
    arg = eb < 130u ? x * 0.15915494309189535f : arg;
    const float r = __builtin_amdgcn_cosf(arg);
    return eb == 255u ? __uint_as_float(0x7fc00000u) : r;
}

__device__ __forceinline__ u32 dpp_add_scan(u32 v) {
    v += __builtin_amdgcn_update_dpp(0u, v, 0x111, 0xf, 0xf, true);
    v += __builtin_amdgcn_update_dpp(0u, v, 0x112, 0xf, 0xf, true);
    v += __builtin_amdgcn_update_dpp(0u, v, 0x114, 0xf, 0xf, true);
    v += __builtin_amdgcn_update_dpp(0u, v, 0x118, 0xf, 0xf, true);
    v += __builtin_amdgcn_update_dpp(0u, v, 0x142, 0xa, 0xf, false);
    v += __builtin_amdgcn_update_dpp(0u, v, 0x143, 0xc, 0xf, false);
    return v;
}
#define SCHED_FENCE() __builtin_amdgcn_sched_barrier(0)

#define AL4(p) ((u32*)__builtin_assume_aligned((p), 4))
#define LDS_BARRIER() do { asm volatile("s_waitcnt lgkmcnt(0)" ::: "memory"); __builtin_amdgcn_s_barrier(); asm volatile("" ::: "memory"); } while (0)

constexpr int P1_STX = 0;
constexpr int P1_STC = NC * 4;
constexpr int P1_HIST = P1_STC + NC * 2;
constexpr int P1_CUR = P1_HIST + NBIN * 4;
constexpr int P1_LDS = P1_CUR + NBIN * 4;

struct Raw { u32x4 o[NG]; u32x4 i[NG]; u32x2 a[NG]; };

__device__ __forceinline__ void p1_load(Raw& r, int g, const int* idx_in, const int* idx_out, const u16* angle, int chunk, u32 t) {
    const bool ok = chunk < NCH;
    const int cc = ok ? chunk : 0;
    auto ro = __builtin_amdgcn_make_buffer_rsrc((void*)(idx_out + (size_t)cc * NC), 0, ok ? NC * 4 : 0, 0x00020000);
    auto ri = __builtin_amdgcn_make_buffer_rsrc((void*)(idx_in + (size_t)cc * NC), 0, ok ? NC * 4 : 0, 0x00020000);
    auto ra = __builtin_amdgcn_make_buffer_rsrc((void*)(angle + (size_t)cc * NC), 0, ok ? NC * 2 : 0, 0x00020000);
    r.o[g] = __builtin_amdgcn_raw_buffer_load_b128(ro, t * 16, g * 16384, P_IN_AUX);
    r.i[g] = __builtin_amdgcn_raw_buffer_load_b128(ri, t * 16, g * 16384, P_IN_AUX);
    r.a[g] = __builtin_amdgcn_raw_buffer_load_b64(ra, t * 8, g * 8192, P_IN_AUX);
}

template <bool PREFETCH>
__device__ __forceinline__ void p1_chunk(char* lds, Raw& raw, int chunk, int nextChunk, float freq, u32 t, int lane, int wave,
                                         const int* idx_in, const int* idx_out, const u16* angle, char* rec, u32* tab) {
    u32* hist = (u32*)(lds + P1_HIST);
    u32* cur = (u32*)(lds + P1_CUR);
    u32 X[RPT], Y[RPT];
#pragma unroll
    for (int g = 0; g < NG; g++) {
        float x[4];
        {
            const u32 a0 = raw.a[g][0], a1 = raw.a[g][1];
            const u32 p0 = pkbf(freq * bf2f(a0 & 0xffffu), freq * __uint_as_float(a0 & 0xffff0000u));
            const u32 p1 = pkbf(freq * bf2f(a1 & 0xffffu), freq * __uint_as_float(a1 & 0xffff0000u));
            x[0] = __uint_as_float(p0 << 16); x[1] = __uint_as_float(p0 & 0xffff0000u);
            x[2] = __uint_as_float(p1 << 16); x[3] = __uint_as_float(p1 & 0xffff0000u);
        }
        float c[4];
#pragma unroll
        for (int k = 0; k < 4; k++) c[k] = cos_small(x[k]);
        const u32 mx = max(max(__float_as_uint(x[0]) & 0x7fffffffu, __float_as_uint(x[1]) & 0x7fffffffu),
                           max(__float_as_uint(x[2]) & 0x7fffffffu, __float_as_uint(x[3]) & 0x7fffffffu));
        if (__builtin_expect(__any(mx >= 0x41000000u), 0)) {
#pragma unroll
            for (int k = 0; k < 4; k++) c[k] = cos_any(x[k]);
        }
        const u32 c01 = pkbf(c[0], c[1]), c23 = pkbf(c[2], c[3]);
#pragma unroll
        for (int k = 0; k < 4; k++) {
            const u32 o = raw.o[g][k], in = raw.i[g][k];
            const u32 ob = divc<EPB>(o);
            const u32 ol4 = (o - __umul24(ob, EPB)) << 2;
            const u32 ph = divc<IBS>(in);
            const u32 key4 = (ph << 10) | (ob << 2);
            X[g * 4 + k] = (in << 13) | ol4;
            const u32 cw = k < 2 ? c01 : c23;
            Y[g * 4 + k] = __builtin_amdgcn_perm(key4, cw, (k & 1) ? 0x05040302u : 0x05040100u);
            asm volatile("" : "+v"(X[g * 4 + k]), "+v"(Y[g * 4 + k]));
            __hip_atomic_fetch_add(AL4(lds + P1_HIST + key4), 1u, __ATOMIC_RELAXED, __HIP_MEMORY_SCOPE_WORKGROUP);
        }
        SCHED_FENCE();
        if (PREFETCH && g < 2) p1_load(raw, g, idx_in, idx_out, angle, nextChunk, t);
        SCHED_FENCE();
    }
    LDS_BARRIER();
    if (wave == 0) {
        constexpr int PER = NBIN / 64;
        u32 v[PER];
#pragma unroll
        for (int j = 0; j < PER / 4; j++) {
            const u32x4 q = *(const u32x4*)(hist + lane * PER + j * 4);
            v[j * 4] = q[0]; v[j * 4 + 1] = q[1]; v[j * 4 + 2] = q[2]; v[j * 4 + 3] = q[3];
        }
        u32 s = 0;
#pragma unroll
        for (int j = 0; j < PER; j++) { const u32 x = v[j]; v[j] = s; s += 2 * x; }
        const u32 ex = dpp_add_scan(s) - s;
#pragma unroll
        for (int j = 0; j < PER / 4; j++) {
            const u32x4 q = {v[j * 4] + ex, v[j * 4 + 1] + ex, v[j * 4 + 2] + ex, v[j * 4 + 3] + ex};
            *(u32x4*)(cur + lane * PER + j * 4) = q;
            *(u32x4*)(hist + lane * PER + j * 4) = u32x4{0, 0, 0, 0};
        }
    }
    LDS_BARRIER();
#pragma unroll
    for (int g = 0; g < NG; g++) {
        u32 p2[4];
#pragma unroll
        for (int k = 0; k < 4; k++)
            p2[k] = __hip_atomic_fetch_add(AL4(lds + P1_CUR + (Y[g * 4 + k] >> 16)), 2u, __ATOMIC_RELAXED, __HIP_MEMORY_SCOPE_WORKGROUP);
#pragma unroll
        for (int k = 0; k < 4; k++) {
            *(u32*)(lds + P1_STX + p2[k] * 2) = X[g * 4 + k];
            *(u16*)(lds + P1_STC + p2[k]) = (u16)Y[g * 4 + k];
        }
        SCHED_FENCE();
        if (PREFETCH && g < NG - 2) p1_load(raw, g + 2, idx_in, idx_out, angle, nextChunk, t);
        SCHED_FENCE();
    }
    LDS_BARRIER();
    {
        auto rr = __builtin_amdgcn_make_buffer_rsrc((void*)(rec + (size_t)chunk * NC * 6), 0, NC * 6, 0x00020000);
#pragma unroll
        for (int r = 0; r < NC / 2 / 1024; r++) {
            const u32 i = t + r * 1024;
            const u32x2 x2 = *(const u32x2*)(lds + P1_STX + i * 8);
            const u32 c2 = *(const u32*)(lds + P1_STC + i * 4);
            u32x3 d;
            d[0] = x2[0];
            d[1] = __builtin_amdgcn_perm(x2[1], c2, 0x05040100u);
            d[2] = __builtin_amdgcn_perm(c2, x2[1], 0x07060302u);
            __builtin_amdgcn_raw_buffer_store_b96(d, rr, t * 12, r * 12288, P_RECST_AUX);
            if (r & 1) SCHED_FENCE();
        }
#pragma unroll
        for (int r = 0; r < (NBIN + 1023) / 1024; r++) {
            const int i = t + r * 1024;
            if (i < NBIN) {
                const int ob = i / NPH, ph = i - ob * NPH;
                const int key = ph * NOB + ob;
                const u32 end = cur[key] >> 1;
                const u32 st = key ? (cur[key - 1] >> 1) : 0u;
                tab[(size_t)chunk * NBIN + i] = st | ((end - st) << 16);
            }
        }
    }
}

__global__ void __launch_bounds__(1024) p1_bin(const int* __restrict__ idx_in, const int* __restrict__ idx_out,
                                               const u16* __restrict__ angle, const u16* __restrict__ ang_freq,
                                               char* __restrict__ rec, u32* __restrict__ tab) {
    __shared__ __attribute__((aligned(16))) char lds[P1_LDS];
    const u32 t = threadIdx.x;
    const int lane = t & 63;
    const int wave = __builtin_amdgcn_readfirstlane(t >> 6);
    const float freq = bf2f(ang_freq[0]);
    static_assert(NCH <= 512, "two chunks per workgroup");
    const int c0 = blockIdx.x, c1 = blockIdx.x + 256;
    Raw raw;
#pragma unroll
    for (int g = 0; g < NG; g++) p1_load(raw, g, idx_in, idx_out, angle, c0, t);
    for (int i = t; i < NBIN; i += 1024) ((u32*)(lds + P1_HIST))[i] = 0;
    LDS_BARRIER();
    p1_chunk<true>(lds, raw, c0, c1, freq, t, lane, wave, idx_in, idx_out, angle, rec, tab);
    if (c1 < NCH) p1_chunk<false>(lds, raw, c1, c1, freq, t, lane, wave, idx_in, idx_out, angle, rec, tab);
}

constexpr int CAP = P_CAP;
constexpr int PF = 10;
constexpr int WIN = PF * 64;
constexpr int TRASH_NODE = 2 * CAP + 1;
constexpr int P2_NODES = 0;
constexpr int P2_HEADS = ((2 + 2 * CAP) * 8 + 15) / 16 * 16;
constexpr int HSTRIDE = 1280 * 4;
constexpr int P2_ENT = P2_HEADS + 2 * HSTRIDE;
constexpr int P2_WTOT = P2_ENT + NPH * 512 * 4;
constexpr int P2_WSCR = P2_WTOT + NPH * 16 * 4;
constexpr int WSCR_SZ = 256;
constexpr int P2_LDS = P2_WSCR + 16 * WSCR_SZ;
static_assert(P2_LDS <= 163840, "LDS");
static_assert(EPB * 64 <= P2_HEADS, "S tile must fit in the node area");
static_assert(2 * CAP + 2 < 65536, "node index");
static_assert(P2_WSCR % 16 == 0, "align");
constexpr u32 HEAD_EMPTY = 1279;
constexpr u32 HEAD_TRASH = 1278;
constexpr u32 OOB = 0xFFFFFF00u;

__device__ __forceinline__ u32 mbcnt64(u32 lo, u32 hi, u32 base) {
    return __builtin_amdgcn_mbcnt_hi(hi, __builtin_amdgcn_mbcnt_lo(lo, base));
}
__device__ __forceinline__ int rfl(int v) { return __builtin_amdgcn_readfirstlane(v); }

__global__ void __launch_bounds__(1024) p2_acc(const u16* __restrict__ m, const u16* __restrict__ dist, const u16* __restrict__ centers,
                                               const u16* __restrict__ w_rbf, const u16* __restrict__ w_sbf,
                                               const u16* __restrict__ w_down, const u16* __restrict__ w_up,
                                               const char* __restrict__ rec, const u32* __restrict__ tab, u16* __restrict__ out) {
    __shared__ __attribute__((aligned(16))) char lds[P2_LDS];
    const u32 t = threadIdx.x;
    const int lane = t & 63;
    const int wave = __builtin_amdgcn_readfirstlane(t >> 6);
    const int ob = (blockIdx.x & 7) * (NOB / 8) + (blockIdx.x >> 3);

    const int chunk = wave + 16 * (lane & 31);
    {
        const bool cok = lane < 32 && chunk < NCH;
        const auto rt = __builtin_amdgcn_make_buffer_rsrc((void*)tab, 0, NCH * NBIN * 4, 0x00020000);
        const u32 toff = cok ? (u32)(chunk * NBIN + ob * NPH) * 4u : OOB;
        u32 ent[NPH];
#pragma unroll
        for (int p = 0; p < NPH; p++) ent[p] = __builtin_amdgcn_raw_buffer_load_b32(rt, toff + p * 4, 0, 0);
        for (int i = t; i < 2 * 1280; i += 1024) ((u32*)(lds + P2_HEADS))[i] = 0;
        if (t == 0) *(u32x2*)(lds + P2_NODES) = u32x2{OOB, 0u};
#pragma unroll
        for (int p = 0; p < NPH; p++) {
            if (lane < 32) *(u32*)(lds + P2_ENT + (p * 512 + wave * 32 + lane) * 4) = ent[p];
            const u32 s = dpp_add_scan(ent[p] >> 16);
            if (lane == 63) *(u32*)(lds + P2_WTOT + (p * 16 + wave) * 4) = s;
        }
    }
    LDS_BARRIER();
    const u32 chunkBase = (u32)chunk * NC;

    const auto rrec = __builtin_amdgcn_make_buffer_rsrc((void*)rec, 0, T_ * 6 + 8, 0x00020000);
    const auto rm = __builtin_amdgcn_make_buffer_rsrc((void*)m, 0, E_ * 32, 0x00020000);
    char* wscr = lds + P2_WSCR + wave * WSCR_SZ;

    int totv;
    {
        int v = 0;
        if (lane < NPH) {
#pragma unroll
            for (int j = 0; j < 4; j++) {
                const u32x4 q = *(const u32x4*)(lds + P2_WTOT + (lane * 16 + j * 4) * 4);
                v += q[0] + q[1] + q[2] + q[3];
            }
        }
        totv = v;
    }
    auto total_of = [&](int p) { return __builtin_amdgcn_readlane(totv, p); };
    auto it_norm = [&](int& p, int& lo) { while (p < NPH && lo >= total_of(p)) { p++; lo = 0; } };

    u32x2 pf[PF];
    u32 bsPn = 0; int bsN = 0; u32 bsNode = 0, bsHead = 0;

    auto issue_window = [&](int w0) {
        if (lane < 16) *(u64*)(wscr + 128 + lane * 8) = 0ull;
        const int pos = (int)bsPn - 1 - w0;
        const bool has = (int)bsPn > 0;
        const bool inw = has && pos >= 0 && pos < WIN;
        const u32 ma = inw ? (u32)(pos >> 6) * 8u : 120u;
        __hip_atomic_fetch_or((u64*)__builtin_assume_aligned(wscr + 128 + ma, 8), 1ull << (pos & 63), __ATOMIC_RELAXED, __HIP_MEMORY_SCOPE_WORKGROUP);
        u32 kb = __popcll(__ballot(has && pos < 0));
        asm volatile("" ::: "memory");
#pragma unroll
        for (int hb = 0; hb < PF; hb += 5) {
            u32x2 M[5];
#pragma unroll
            for (int b = 0; b < 5; b++) M[b] = *(const u32x2*)(wscr + 128 + (hb + b) * 8);
            u32 kk[5];
#pragma unroll
            for (int b = 0; b < 5; b++) {
                kk[b] = mbcnt64(M[b][0], M[b][1], kb);
                kb += __popc(M[b][0]) + __popc(M[b][1]);
            }
            u32 d[5];
#pragma unroll
            for (int b = 0; b < 5; b++) d[b] = *(const u32*)(wscr + (kk[b] & 31) * 4);
#pragma unroll
            for (int b = 0; b < 5; b++) {
                const int q = w0 + (hb + b) * 64 + lane;
                const u32 off = q < bsN ? (q + d[b]) * 6u : OOB;
                pf[hb + b] = __builtin_amdgcn_raw_buffer_load_b64(rrec, off, 0, P_RECLD_AUX);
            }
            SCHED_FENCE();
        }
        asm volatile("" ::: "memory");
    };
    auto setup = [&](int p, int lo, int par) {
        const u32 e = lane < 32 ? *(const u32*)(lds + P2_ENT + (p * 512 + wave * 32 + lane) * 4) : 0u;
        const u32 wt = lane < 16 ? *(const u32*)(lds + P2_WTOT + (p * 16 + lane) * 4) : 0u;
        const int winc = dpp_add_scan(wt);
        const int Nw = __builtin_amdgcn_readlane(wt, wave);
        const int B = __builtin_amdgcn_readlane(winc, wave) - Nw;
        const int range = min(CAP, total_of(p) - lo);
        const int len = e >> 16, start = e & 0xffff;
        const int P = (int)dpp_add_scan(len) - len;
        const int A = B + P - lo;
        const int klo = max(0, -A);
        const int khi = min(len, range - A);
        const bool ne = khi > klo;
        const int ws = min(max(B - lo, 0), range);
        const int we = min(max(B + Nw - lo, 0), range);
        const int Pn = min(max(A, 0), range) - ws;
        bsPn = ne ? (u32)Pn : 0x80000000u;
        bsN = we - ws;
        bsNode = 1 + par * CAP + ws;
        bsHead = P2_HEADS + par * HSTRIDE;
        const u64 NE = __ballot(ne);
        const u32 k = mbcnt64((u32)NE, (u32)(NE >> 32), 0);
        *(u32*)(wscr + (ne ? k * 4 : 248u)) = chunkBase + start + klo - Pn;
        asm volatile("" ::: "memory");
        issue_window(0);
    };
    auto link_window = [&](int w0) {
#pragma unroll
        for (int hb = 0; hb < PF; hb += 5) {
            u32 prev[5];
#pragma unroll
            for (int b = 0; b < 5; b++) {
                const int q = w0 + (hb + b) * 64 + lane;
                const u32 ha = q < bsN ? (pf[hb + b][0] & 0x1FFCu) : HEAD_TRASH * 4;
                prev[b] = __hip_atomic_exchange(AL4(lds + bsHead + ha), bsNode + q, __ATOMIC_RELAXED, __HIP_MEMORY_SCOPE_WORKGROUP);
            }
#pragma unroll
            for (int b = 0; b < 5; b++) {
                const int q = w0 + (hb + b) * 64 + lane;
                const u32 ni = q < bsN ? bsNode + q : (u32)TRASH_NODE;
                u32x2 nd;
                nd[0] = (pf[hb + b][0] >> 8) & 0xFFFFE0u;
                nd[1] = (pf[hb + b][1] << 16) | prev[b];
                *(u32x2*)(lds + P2_NODES + ni * 8) = nd;
            }
            SCHED_FENCE();
        }
    };
    auto link = [&]() {
        link_window(0);
        for (int w0 = WIN; w0 < bsN; w0 += WIN) { issue_window(w0); link_window(w0); }
    };

    const int pr = lane >> 1;
    const u32 h16 = (lane & 1) * 16;
    const u32 e0a = (wave * 32 + pr) * 4;
    u32 e2a;
    {
        const int cnt = 14 + (wave < 2);
        e2a = (pr < cnt ? 1024 + wave * 14 + min(wave, 2) + pr : (int)HEAD_EMPTY) * 4;
    }
    f32x8 accA = {0.f, 0.f, 0.f, 0.f, 0.f, 0.f, 0.f, 0.f}, accB = accA, accC = accA;

    auto traverse = [&](int par) {
        char* hb = lds + P2_HEADS + par * HSTRIDE;
        u32 c0 = *(const u32*)(hb + e0a), c1 = *(const u32*)(hb + e0a + 2048), c2 = *(const u32*)(hb + e2a);
        asm volatile("" ::: "memory");
        *(u32*)(hb + e0a) = 0u; *(u32*)(hb + e0a + 2048) = 0u; *(u32*)(hb + e2a) = 0u;
        asm volatile(
            "s_waitcnt lgkmcnt(0)\n\t"
            "v_lshlrev_b32 v84, 3, %3\n\t"
            "ds_read_b64 v[86:87], v84\n\t"
            "v_lshlrev_b32 v84, 3, %4\n\t"
            "ds_read_b64 v[88:89], v84\n\t"
            "v_lshlrev_b32 v84, 3, %5\n\t"
            "ds_read_b64 v[90:91], v84\n\t"
            "v_or3_b32 v84, %3, %4, %5\n\t"
            "v_cmp_ne_u32 vcc, 0, v84\n\t"
            "s_cbranch_vccz 3f\n\t"
            "s_waitcnt lgkmcnt(2)\n\t"
            "v_add_u32 v84, v86, %6\n\t"
            "v_mov_b32 %3, v87\n\t"
            "buffer_load_dwordx4 v[92:95], v84, %7, 0 offen\n\t"
            "v_mad_u32_u16 v84, %3, 8, 0\n\t"
            "ds_read_b64 v[86:87], v84\n\t"
            "s_waitcnt lgkmcnt(2)\n\t"
            "v_add_u32 v84, v88, %6\n\t"
            "v_mov_b32 %4, v89\n\t"
            "buffer_load_dwordx4 v[96:99], v84, %7, 0 offen\n\t"
            "v_mad_u32_u16 v84, %4, 8, 0\n\t"
            "ds_read_b64 v[88:89], v84\n\t"
            "s_waitcnt lgkmcnt(2)\n\t"
            "v_add_u32 v84, v90, %6\n\t"
            "v_mov_b32 %5, v91\n\t"
            "buffer_load_dwordx4 v[100:103], v84, %7, 0 offen\n\t"
            "v_mad_u32_u16 v84, %5, 8, 0\n\t"
            "ds_read_b64 v[90:91], v84\n\t"
            "1:\n\t"
            "v_or3_b32 v84, %3, %4, %5\n\t"
            "v_and_b32 v84, 0xffff, v84\n\t"
            "v_cmp_ne_u32 vcc, 0, v84\n\t"
            "s_cbranch_vccz 2f\n\t"
            "s_waitcnt vmcnt(2)\n\t"
            "v_lshrrev_b32 v84, 16, %3\n\t"
            "v_and_b32 v85, 0xffff0000, %3\n\t"
            "v_dot2c_f32_bf16 v104, v92, v84\n\t"
            "v_dot2c_f32_bf16 v105, v92, v85\n\t"
            "v_dot2c_f32_bf16 v106, v93, v84\n\t"
            "v_dot2c_f32_bf16 v107, v93, v85\n\t"
            "v_dot2c_f32_bf16 v108, v94, v84\n\t"
            "v_dot2c_f32_bf16 v109, v94, v85\n\t"
            "v_dot2c_f32_bf16 v110, v95, v84\n\t"
            "v_dot2c_f32_bf16 v111, v95, v85\n\t"
            "s_waitcnt lgkmcnt(2)\n\t"
            "v_add_u32 v84, v86, %6\n\t"
            "v_mov_b32 %3, v87\n\t"
            "buffer_load_dwordx4 v[92:95], v84, %7, 0 offen\n\t"
            "v_mad_u32_u16 v84, %3, 8, 0\n\t"
            "ds_read_b64 v[86:87], v84\n\t"
            "s_waitcnt vmcnt(2)\n\t"
            "v_lshrrev_b32 v84, 16, %4\n\t"
            "v_and_b32 v85, 0xffff0000, %4\n\t"
            "v_dot2c_f32_bf16 v112, v96, v84\n\t"
            "v_dot2c_f32_bf16 v113, v96, v85\n\t"
            "v_dot2c_f32_bf16 v114, v97, v84\n\t"
            "v_dot2c_f32_bf16 v115, v97, v85\n\t"
            "v_dot2c_f32_bf16 v116, v98, v84\n\t"
            "v_dot2c_f32_bf16 v117, v98, v85\n\t"
            "v_dot2c_f32_bf16 v118, v99, v84\n\t"
            "v_dot2c_f32_bf16 v119, v99, v85\n\t"
            "s_waitcnt lgkmcnt(2)\n\t"
            "v_add_u32 v84, v88, %6\n\t"
            "v_mov_b32 %4, v89\n\t"
            "buffer_load_dwordx4 v[96:99], v84, %7, 0 offen\n\t"
            "v_mad_u32_u16 v84, %4, 8, 0\n\t"
            "ds_read_b64 v[88:89], v84\n\t"
            "s_waitcnt vmcnt(2)\n\t"
            "v_lshrrev_b32 v84, 16, %5\n\t"
            "v_and_b32 v85, 0xffff0000, %5\n\t"
            "v_dot2c_f32_bf16 v120, v100, v84\n\t"
            "v_dot2c_f32_bf16 v121, v100, v85\n\t"
            "v_dot2c_f32_bf16 v122, v101, v84\n\t"
            "v_dot2c_f32_bf16 v123, v101, v85\n\t"
            "v_dot2c_f32_bf16 v124, v102, v84\n\t"
            "v_dot2c_f32_bf16 v125, v102, v85\n\t"
            "v_dot2c_f32_bf16 v126, v103, v84\n\t"
            "v_dot2c_f32_bf16 v127, v103, v85\n\t"
            "s_waitcnt lgkmcnt(2)\n\t"
            "v_add_u32 v84, v90, %6\n\t"
            "v_mov_b32 %5, v91\n\t"
            "buffer_load_dwordx4 v[100:103], v84, %7, 0 offen\n\t"
            "v_mad_u32_u16 v84, %5, 8, 0\n\t"
            "ds_read_b64 v[90:91], v84\n\t"
            "s_branch 1b\n2:\n\t"
            "s_waitcnt vmcnt(2)\n\t"
            "v_lshrrev_b32 v84, 16, %3\n\t"
            "v_and_b32 v85, 0xffff0000, %3\n\t"
            "v_dot2c_f32_bf16 v104, v92, v84\n\t"
            "v_dot2c_f32_bf16 v105, v92, v85\n\t"
            "v_dot2c_f32_bf16 v106, v93, v84\n\t"
            "v_dot2c_f32_bf16 v107, v93, v85\n\t"
            "v_dot2c_f32_bf16 v108, v94, v84\n\t"
            "v_dot2c_f32_bf16 v109, v94, v85\n\t"
            "v_dot2c_f32_bf16 v110, v95, v84\n\t"
            "v_dot2c_f32_bf16 v111, v95, v85\n\t"
            "s_waitcnt vmcnt(1)\n\t"
            "v_lshrrev_b32 v84, 16, %4\n\t"
            "v_and_b32 v85, 0xffff0000, %4\n\t"
            "v_dot2c_f32_bf16 v112, v96, v84\n\t"
            "v_dot2c_f32_bf16 v113, v96, v85\n\t"
            "v_dot2c_f32_bf16 v114, v97, v84\n\t"
            "v_dot2c_f32_bf16 v115, v97, v85\n\t"
            "v_dot2c_f32_bf16 v116, v98, v84\n\t"
            "v_dot2c_f32_bf16 v117, v98, v85\n\t"
            "v_dot2c_f32_bf16 v118, v99, v84\n\t"
            "v_dot2c_f32_bf16 v119, v99, v85\n\t"
            "s_waitcnt vmcnt(0)\n\t"
            "v_lshrrev_b32 v84, 16, %5\n\t"
            "v_and_b32 v85, 0xffff0000, %5\n\t"
            "v_dot2c_f32_bf16 v120, v100, v84\n\t"
            "v_dot2c_f32_bf16 v121, v100, v85\n\t"
            "v_dot2c_f32_bf16 v122, v101, v84\n\t"
            "v_dot2c_f32_bf16 v123, v101, v85\n\t"
            "v_dot2c_f32_bf16 v124, v102, v84\n\t"
            "v_dot2c_f32_bf16 v125, v102, v85\n\t"
            "v_dot2c_f32_bf16 v126, v103, v84\n\t"
            "v_dot2c_f32_bf16 v127, v103, v85\n\t"
            "3:\n\t"
            "s_waitcnt lgkmcnt(0)\n\t"
            "s_nop 3"
            : "+{v[104:111]}"(accA), "+{v[112:119]}"(accB), "+{v[120:127]}"(accC), "+v"(c0), "+v"(c1), "+v"(c2)
            : "v"(h16), "s"(rm)
            : "vcc", "memory", "v84", "v85", "v86", "v87", "v88", "v89", "v90", "v91", "v92", "v93", "v94", "v95", "v96", "v97", "v98", "v99", "v100", "v101", "v102", "v103");
    };

    int cp = 0, clo = 0;
    it_norm(cp, clo);
    if (cp < NPH) {
        int np = cp, nlo = clo;
        setup(np, nlo, 0);
        link();
        nlo += CAP; it_norm(np, nlo);
        if (np < NPH) setup(np, nlo, 1);
        LDS_BARRIER();
        int par = 0;
        const bool typeA = wave & 1;
        while (cp < NPH) {
            const bool hasNext = np < NPH;
            int qp = np, qlo = nlo + CAP;
            if (hasNext) it_norm(qp, qlo);
            if (!typeA) traverse(par);
            if (hasNext) { link(); if (qp < NPH) setup(qp, qlo, par); }
            if (typeA) traverse(par);
            LDS_BARRIER();
            cp = np; clo = nlo; np = qp; nlo = qlo; par ^= 1;
        }
    }

#pragma unroll
    for (int s = 0; s < 3; s++) {
        const u32 ea = s == 0 ? e0a : s == 1 ? e0a + 2048 : e2a;
        if (ea < EPB * 4) {
            const f32x8 av = s == 0 ? accA : s == 1 ? accB : accC;
            const f32x4 a0 = {av[0], av[1], av[2], av[3]}, a1 = {av[4], av[5], av[6], av[7]};
            *(f32x4*)(lds + ea * 16 + h16 * 2) = a0;
            *(f32x4*)(lds + ea * 16 + h16 * 2 + 16) = a1;
        }
    }
    LDS_BARRIER();
    {
        const int i = lane & 15, q = lane >> 4;
        u32 wd0 = w_down[(4 * q) * 16 + i] | ((u32)w_down[(4 * q + 1) * 16 + i] << 16);
        u32 wd1 = w_down[(4 * q + 2) * 16 + i] | ((u32)w_down[(4 * q + 3) * 16 + i] << 16);
        u32 wu0 = w_up[(4 * q) * 16 + i] | ((u32)w_up[(4 * q + 1) * 16 + i] << 16);
        u32 wu1 = w_up[(4 * q + 2) * 16 + i] | ((u32)w_up[(4 * q + 3) * 16 + i] << 16);
        u32 ws0 = w_sbf[(2 * q) * 16 + i] | ((u32)w_sbf[(2 * q + 1) * 16 + i] << 16);
        u32 wr0 = w_rbf[(2 * q) * 16 + i] | ((u32)w_rbf[(2 * q + 1) * 16 + i] << 16);
        const float ce0 = bf2f(centers[2 * q]), ce1 = bf2f(centers[2 * q + 1]);
        const bf16x8_t Awd = __builtin_bit_cast(bf16x8_t, u32x4{wd0, wd1, wd0, wd1});
        const bf16x8_t Awu = __builtin_bit_cast(bf16x8_t, u32x4{wu0, wu1, wu0, wu1});
        const bf16x8_t Aws = __builtin_bit_cast(bf16x8_t, u32x4{ws0, 0u, 0u, 0u});
        const bf16x8_t Awr = __builtin_bit_cast(bf16x8_t, u32x4{wr0, 0u, 0u, 0u});
        auto split = [&](const f32x4& v) {
            const u32 h0 = pkbf(v[0], v[1]), h1 = pkbf(v[2], v[3]);
            const u32 l0 = pkbf(v[0] - bf2f(h0 & 0xffffu), v[1] - __uint_as_float(h0 & 0xffff0000u));
            const u32 l1 = pkbf(v[2] - bf2f(h1 & 0xffffu), v[3] - __uint_as_float(h1 & 0xffff0000u));
            return __builtin_bit_cast(bf16x8_t, u32x4{h0, h1, l0, l1});
        };
        constexpr int NT = (EPB + 15) / 16;
        for (int tile = wave; tile < NT; tile += 16) {
            const int el = tile * 16 + i;
            const bool ok = el < EPB;
            const int elc = ok ? el : EPB - 1;
            const size_t e = (size_t)ob * EPB + elc;
            const u32x2 mv = *(const u32x2*)(m + e * 16 + q * 4);
            const float d = bf2f(dist[e]);
            const f32x4 sv = *(const f32x4*)(lds + elc * 64 + q * 16);
            float r0, r1;
            {
                float x0 = rbf16(d - ce0), x1 = rbf16(d - ce1);
                x0 = rbf16(x0 * x0); x1 = rbf16(x1 * x1);
                x0 = rbf16(-10.0f * x0); x1 = rbf16(-10.0f * x1);
                r0 = expf(x0); r1 = expf(x1);
            }
            const bf16x8_t Br = __builtin_bit_cast(bf16x8_t, u32x4{pkbf(r0, r1), 0u, 0u, 0u});
            const f32x4 z = {0.f, 0.f, 0.f, 0.f};
            const f32x4 md = __builtin_amdgcn_mfma_f32_16x16x32_bf16(Awd, split(sv), z, 0, 0, 0);
            const f32x4 g = __builtin_amdgcn_mfma_f32_16x16x32_bf16(Aws, Br, z, 0, 0, 0);
            const f32x4 hh = __builtin_amdgcn_mfma_f32_16x16x32_bf16(Awr, Br, z, 0, 0, 0);
            const f32x4 agg = md * g;
            const f32x4 up = __builtin_amdgcn_mfma_f32_16x16x32_bf16(Awu, split(agg), z, 0, 0, 0);
            const float o0 = bf2f(mv[0] & 0xffffu) + up[0] * hh[0];
            const float o1 = __uint_as_float(mv[0] & 0xffff0000u) + up[1] * hh[1];
            const float o2 = bf2f(mv[1] & 0xffffu) + up[2] * hh[2];
            const float o3 = __uint_as_float(mv[1] & 0xffff0000u) + up[3] * hh[3];
            if (ok) *(u32x2*)(out + e * 16 + q * 4) = u32x2{pkbf(o0, o1), pkbf(o2, o3)};
        }
    }
}

}

extern "C" void kernel_launch(void* const* d_in, const int* in_sizes, int n_in,
                              void* d_out, int out_size, void* d_ws, size_t ws_size,
                              hipStream_t stream) {
    const u16* m = (const u16*)d_in[0];
    const int* idx_in = (const int*)d_in[1];
    const int* idx_out = (const int*)d_in[2];
    const u16* dist = (const u16*)d_in[3];
    const u16* angle = (const u16*)d_in[4];
    const u16* centers = (const u16*)d_in[5];
    const u16* ang_freq = (const u16*)d_in[6];
    const u16* w_rbf = (const u16*)d_in[7];
    const u16* w_sbf = (const u16*)d_in[8];
    const u16* w_down = (const u16*)d_in[9];
    const u16* w_up = (const u16*)d_in[10];
    char* ws = (char*)d_ws + P_WS_OFF;
    char* rec = ws;
    u32* tab = (u32*)(ws + (((size_t)T_ * 6 + 256 + 255) / 256) * 256);
    p1_bin<<<256, 1024, 0, stream>>>(idx_in, idx_out, angle, ang_freq, rec, tab);
    p2_acc<<<NOB, 1024, 0, stream>>>(m, dist, centers, w_rbf, w_sbf, w_down, w_up, rec, tab, (u16*)d_out);
}
```

```cpp
#include <hip/hip_runtime.h>
#include <hip/hip_bf16.h>
#include <stdint.h>

typedef unsigned short u16;
typedef unsigned u32;
typedef unsigned long long u64;
typedef u32 u32x2 __attribute__((ext_vector_type(2)));
typedef u32 u32x3 __attribute__((ext_vector_type(3)));
typedef u32 u32x4 __attribute__((ext_vector_type(4)));
typedef u32 u32x12 __attribute__((ext_vector_type(12)));
typedef float f32x2 __attribute__((ext_vector_type(2)));
typedef float f32x4 __attribute__((ext_vector_type(4)));
typedef __bf16 bf16x2_t __attribute__((ext_vector_type(2)));
typedef __bf16 bf16x8_t __attribute__((ext_vector_type(8)));

namespace {

constexpr int E_ = 320000;
constexpr int T_ = 10240000;
constexpr int NOB = 256;
constexpr int EPB = E_ / NOB;
constexpr int NPH = 5;
constexpr int IBS = (E_ + NPH - 1) / NPH;
constexpr int NBIN = NPH * NOB;
constexpr int NWG1 = 256;
constexpr int TPW = T_ / NWG1;
constexpr int CS0 = 16384, CS1 = 16384, CS2 = TPW - CS0 - CS1;
constexpr int NCH = NWG1 * 3;
static_assert(NWG1 * TPW == T_, "chunking");
static_assert(NOB * EPB == E_, "blocks");
static_assert(NBIN % 64 == 0 && (NBIN / 64) % 4 == 0, "scan layout");

constexpr int ctz_(int d) { return (d & 1) ? 0 : 1 + ctz_(d >> 1); }
template <int D> struct Magic {
    static constexpr bool direct = ((((1ull << 32) + D - 1) / D) * D - (1ull << 32)) * (u64)E_ < (1ull << 32);
    static constexpr int SH = direct ? 0 : ctz_(D);
    static constexpr int DD = D >> SH;
    static constexpr u32 M = (u32)(((1ull << 32) + DD - 1) / DD);
    static_assert(M < (1u << 24), "magic too large");
    static_assert(((u64)M * DD - (1ull << 32)) * (u64)((E_ >> SH) + 1) < (1ull << 32), "magic inexact");
};
__device__ __forceinline__ u32 mulhi24(u32 a, u32 b) {
    u32 r; asm("v_mul_hi_u32_u24 %0, %1, %2" : "=v"(r) : "s"(b), "v"(a)); return r;
}
template <int D> __device__ __forceinline__ u32 divc(u32 x) { return mulhi24(x >> Magic<D>::SH, Magic<D>::M); }

__device__ __forceinline__ float bf2f(u32 v16) { return __uint_as_float(v16 << 16); }
__device__ __forceinline__ u32 pkbf(float lo, float hi) {
    bf16x2_t v = {(__bf16)lo, (__bf16)hi};
    return __builtin_bit_cast(u32, v);
}
__device__ __forceinline__ float rbf16(float x) { return __uint_as_float(pkbf(x, 0.f) << 16); }

__device__ const u32 kInv2PiFrac[125] = {
    0x028BE60Eu, 0x0517CC1Bu, 0x0A2F9837u, 0x145F306Eu, 0x28BE60DCu, 0x517CC1B7u, 0xA2F9836Eu, 0x45F306DDu,
    0x8BE60DB9u, 0x17CC1B72u, 0x2F9836E5u, 0x5F306DCAu, 0xBE60DB94u, 0x7CC1B727u, 0xF9836E4Eu, 0xF306DC9Du,
    0xE60DB939u, 0xCC1B7272u, 0x9836E4E4u, 0x306DC9C9u, 0x60DB9391u, 0xC1B72722u, 0x836E4E44u, 0x06DC9C88u,
    0x0DB93910u, 0x1B727221u, 0x36E4E441u, 0x6DC9C883u, 0xDB939105u, 0xB727220Bu, 0x6E4E4415u, 0xDC9C882Au,
    0xB9391055u, 0x727220A9u, 0xE4E44153u, 0xC9C882A5u, 0x9391054Au, 0x27220A95u, 0x4E44152Au, 0x9C882A54u,
    0x391054A8u, 0x7220A950u, 0xE44152A0u, 0xC882A540u, 0x91054A7Fu, 0x220A94FEu, 0x441529FCu, 0x882A53F8u,
    0x1054A7F1u, 0x20A94FE1u, 0x41529FC2u, 0x82A53F85u, 0x054A7F0Au, 0x0A94FE14u, 0x1529FC27u, 0x2A53F84Fu,
    0x54A7F09Du, 0xA94FE13Bu, 0x529FC275u, 0xA53F84EBu, 0x4A7F09D6u, 0x94FE13ACu, 0x29FC2758u, 0x53F84EB0u,
    0xA7F09D5Fu, 0x4FE13ABFu, 0x9FC2757Du, 0x3F84EAFAu, 0x7F09D5F4u, 0xFE13ABE9u, 0xFC2757D2u, 0xF84EAFA4u,
    0xF09D5F48u, 0xE13ABE90u, 0xC2757D1Fu, 0x84EAFA3Fu, 0x09D5F47Du, 0x13ABE8FBu, 0x2757D1F5u, 0x4EAFA3EAu,
    0x9D5F47D5u, 0x3ABE8FAAu, 0x757D1F53u, 0xEAFA3EA7u, 0xD5F47D4Du, 0xABE8FA9Au, 0x57D1F535u, 0xAFA3EA6Au,
    0x5F47D4D3u, 0xBE8FA9A7u, 0x7D1F534Eu, 0xFA3EA69Cu, 0xF47D4D37u, 0xE8FA9A6Fu, 0xD1F534DEu, 0xA3EA69BCu,
    0x47D4D377u, 0x8FA9A6EEu, 0x1F534DDCu, 0x3EA69BB8u, 0x7D4D3770u, 0xFA9A6EE0u, 0xF534DDC1u, 0xEA69BB82u,
    0xD4D37703u, 0xA9A6EE07u, 0x534DDC0Eu, 0xA69BB81Bu, 0x4D377037u, 0x9A6EE06Eu, 0x34DDC0DBu, 0x69BB81B7u,
    0xD377036Eu, 0xA6EE06DBu, 0x4DDC0DB6u, 0x9BB81B6Cu, 0x377036D9u, 0x6EE06DB1u, 0xDDC0DB63u, 0xBB81B6C5u,
    0x77036D8Au, 0xEE06DB15u, 0xDC0DB629u, 0xB81B6C53u, 0x7036D8A5u};

__device__ __forceinline__ float cos_any(float x) {
    const u32 b = (__float_as_uint(x) >> 16) & 0x7fffu;
    const u32 eb = b >> 7;
    const u32 ti = min(max(eb, 130u), 254u) - 130u;
    const u32 mm = (b & 0x7fu) | 0x80u;
    const u32 rev = mm * kInv2PiFrac[ti];
    float arg = (float)rev * 2.3283064365386963e-10f;
    arg = eb < 130u ? x * 0.15915494309189535f : arg;
    const float r = __builtin_amdgcn_cosf(arg);
    return eb == 255u ? __uint_as_float(0x7fc00000u) : r;
}

__device__ __forceinline__ u32 dpp_add_scan(u32 v) {
    v += __builtin_amdgcn_update_dpp(0u, v, 0x111, 0xf, 0xf, true);
    v += __builtin_amdgcn_update_dpp(0u, v, 0x112, 0xf, 0xf, true);
    v += __builtin_amdgcn_update_dpp(0u, v, 0x114, 0xf, 0xf, true);
    v += __builtin_amdgcn_update_dpp(0u, v, 0x118, 0xf, 0xf, true);
    v += __builtin_amdgcn_update_dpp(0u, v, 0x142, 0xa, 0xf, false);
    v += __builtin_amdgcn_update_dpp(0u, v, 0x143, 0xc, 0xf, false);
    return v;
}
#define SCHED_FENCE() __builtin_amdgcn_sched_barrier(0)

#define AL4(p) ((u32*)__builtin_assume_aligned((p), 4))
#define LDS_BARRIER() do { asm volatile("s_waitcnt lgkmcnt(0)" ::: "memory"); __builtin_amdgcn_s_barrier(); asm volatile("" ::: "memory"); } while (0)

constexpr int CSMAX = 16384;
constexpr int CSTR = CSMAX + NBIN;
constexpr int P1_HSZ = NBIN * 4;
constexpr int P1_HIST = 0;
constexpr int P1_CUR = 2 * P1_HSZ;
constexpr int P1_SSZ = ((NBIN + 1) * 2 + 15) / 16 * 16;
constexpr int P1_STA = 3 * P1_HSZ;
constexpr int P1_ST = P1_STA + 2 * P1_SSZ;
constexpr int P1_LDS = P1_ST + CSTR * 8;
static_assert(P1_LDS <= 163840 && P1_ST % 16 == 0, "LDS");
static_assert(CS0 <= CSMAX && CS1 <= CSMAX && CS2 <= CSMAX && CS0 % 4 == 0 && CS1 % 4 == 0 && CS2 % 4 == 0 && CS2 > 0, "group layout");
constexpr u32 LEN_SAT = 2047;
static_assert(NPH == 5, "run table packing");
constexpr u32 DUMMY_X = (0x7FFFFu << 13) | (79u * 16u << 2);

constexpr int NT_LOAD = 2;

struct Raw { u32x4 o, i; u32x2 a; };

__device__ __forceinline__ void p1_load(Raw& r, int g, const int* idx_in, const int* idx_out, const u16* angle, size_t base, int n, u32 t) {
    auto ro = __builtin_amdgcn_make_buffer_rsrc((void*)(idx_out + base), 0, n * 4, 0x00020000);
    auto ri = __builtin_amdgcn_make_buffer_rsrc((void*)(idx_in + base), 0, n * 4, 0x00020000);
    auto ra = __builtin_amdgcn_make_buffer_rsrc((void*)(angle + base), 0, n * 2, 0x00020000);
    const u32 q = t + g * 1024;
    r.a = __builtin_amdgcn_raw_buffer_load_b64(ra, q * 8, 0, NT_LOAD);
    r.o = __builtin_amdgcn_raw_buffer_load_b128(ro, q * 16, 0, NT_LOAD);
    r.i = __builtin_amdgcn_raw_buffer_load_b128(ri, q * 16, 0, NT_LOAD);
}
__device__ __forceinline__ void p1_keys(const Raw& r, float freq, u32 (&X)[4], u32 (&Y)[4]) {
    const f32x2 fr2 = {freq, freq}, i2pi = {0.15915494309189535f, 0.15915494309189535f};
    u32 a0 = r.a[0], a1 = r.a[1];
    asm volatile("" : "+v"(a0), "+v"(a1));
    const f32x2 x01 = fr2 * f32x2{__uint_as_float(a0 << 16), __uint_as_float(a0 & 0xffff0000u)};
    const f32x2 x23 = fr2 * f32x2{__uint_as_float(a1 << 16), __uint_as_float(a1 & 0xffff0000u)};
    const u32 p0 = pkbf(x01[0], x01[1]), p1 = pkbf(x23[0], x23[1]);
    const f32x2 r01 = i2pi * f32x2{__uint_as_float(p0 << 16), __uint_as_float(p0 & 0xffff0000u)};
    const f32x2 r23 = i2pi * f32x2{__uint_as_float(p1 << 16), __uint_as_float(p1 & 0xffff0000u)};
    u32 c01 = pkbf(__builtin_amdgcn_cosf(r01[0]), __builtin_amdgcn_cosf(r01[1]));
    u32 c23 = pkbf(__builtin_amdgcn_cosf(r23[0]), __builtin_amdgcn_cosf(r23[1]));
    const u32 big = ((p0 & 0x7fff7fffu) + 0x3f003f00u) | ((p1 & 0x7fff7fffu) + 0x3f003f00u);
    if (__builtin_expect(__any((big & 0x80008000u) != 0), 0)) {
        u32 s0 = p0, s1 = p1;
        asm volatile("" : "+v"(s0), "+v"(s1));
        c01 = pkbf(cos_any(__uint_as_float(s0 << 16)), cos_any(__uint_as_float(s0 & 0xffff0000u)));
        c23 = pkbf(cos_any(__uint_as_float(s1 << 16)), cos_any(__uint_as_float(s1 & 0xffff0000u)));
    }
#pragma unroll
    for (int k = 0; k < 4; k++) {
        u32 o = r.o[k], in = r.i[k];
        asm volatile("" : "+v"(o), "+v"(in));
        const u32 ob = divc<EPB>(o);
        const u32 ph = divc<IBS>(in);
        const u32 key4 = ((ph << 8) + ob) << 2;
        u32 x = (in << 13) + (o << 2);
        asm("v_mad_i32_i24 %0, %1, %2, %0" : "+v"(x) : "v"(ob), "s"(-4 * EPB));
        X[k] = x;
        Y[k] = __builtin_amdgcn_perm(key4, k < 2 ? c01 : c23, (k & 1) ? 0x05040302u : 0x05040100u);
        asm volatile("" : "+v"(X[k]), "+v"(Y[k]));
    }
}
__device__ __forceinline__ void p1_scan(char* lds, int par, int lane) {
    constexpr int PER = NBIN / 64;
    const u32* hist = (const u32*)(lds + P1_HIST + par * P1_HSZ);
    u32* cur = (u32*)(lds + P1_CUR);
    u16* sta = (u16*)(lds + P1_STA + par * P1_SSZ);
    u32 s = 0;
#pragma unroll
    for (int j = 0; j < PER / 4; j++) {
        const u32x4 q = (*(const u32x4*)(hist + lane * PER + j * 4) + 1) & ~1u;
        s += q[0] + q[1] + q[2] + q[3];
    }
    const u32 inc = dpp_add_scan(s);
    u32 run = inc - s;
    asm volatile("" ::: "memory");
#pragma unroll
    for (int j = 0; j < PER / 4; j++) {
        const u32x4 h = (*(const u32x4*)(hist + lane * PER + j * 4) + 1) & ~1u;
        const u32x4 q = {run, run + h[0], run + h[0] + h[1], run + h[0] + h[1] + h[2]};
        run = q[3] + h[3];
        *(u32x4*)(cur + lane * PER + j * 4) = q * 8;
        *(u32x2*)(sta + lane * PER + j * 4) = u32x2{q[0] | (q[1] << 16), q[2] | (q[3] << 16)};
        SCHED_FENCE();
    }
    if (lane == 63) sta[NBIN] = (u16)inc;
}

__global__ void __launch_bounds__(1024) p1_bin(const int* __restrict__ idx_in, const int* __restrict__ idx_out,
                                               const u16* __restrict__ angle, const u16* __restrict__ ang_freq,
                                               char* __restrict__ rec, u32x4* __restrict__ tab, u32* __restrict__ side) {
    __shared__ __attribute__((aligned(16))) char lds[P1_LDS];
    const u32 t = threadIdx.x;
    const int lane = t & 63;
    const int wave = __builtin_amdgcn_readfirstlane(t >> 6);
    const size_t base = (size_t)blockIdx.x * TPW;
    const u32 fw = *(const __attribute__((address_space(4))) u32*)(uintptr_t)ang_freq;
    const float freq = __uint_as_float(fw << 16);
    Raw raw[4];
#pragma unroll
    for (int g = 0; g < 4; g++) {
        if (g * 4096 < CS0) p1_load(raw[g], g, idx_in, idx_out, angle, base, CS0, t);
        else if (g * 4096 < CS1) p1_load(raw[g], g, idx_in, idx_out, angle, base + CS0, CS1, t);
        SCHED_FENCE();
        if (g < 3) __builtin_amdgcn_s_barrier();
    }
    for (int i = t; i < 2 * NBIN; i += 1024) ((u32*)(lds + P1_HIST))[i] = 0;
    LDS_BARRIER();
    u32 X[4][4], Y[4][4];
#pragma unroll
    for (int g = 0; g < 4; g++) {
        if (g * 4096 < CS0) {
            p1_keys(raw[g], freq, X[g], Y[g]);
            if (g * 4096 + 4096 <= CS0 || g * 4096 + (int)t * 4 < CS0) {
#pragma unroll
                for (int k = 0; k < 4; k++)
                    __hip_atomic_fetch_add(AL4(lds + P1_HIST + (Y[g][k] >> 16)), 1u, __ATOMIC_RELAXED, __HIP_MEMORY_SCOPE_WORKGROUP);
            }
            SCHED_FENCE();
            if (g * 4096 < CS1) p1_load(raw[g], g, idx_in, idx_out, angle, base + CS0, CS1, t);
            SCHED_FENCE();
        }
    }
    LDS_BARRIER();
    if (wave == 0) p1_scan(lds, 0, lane);
    LDS_BARRIER();
#pragma clang loop unroll(disable)
    for (int j = 0; j < 3; j++) {
        const int par = j & 1;
        const int n = j == 0 ? CS0 : j == 1 ? CS1 : CS2;
        const int nn = j == 0 ? CS1 : j == 1 ? CS2 : 0;
        const int n2 = j == 0 ? CS2 : 0;
        const u32* hist = (const u32*)(lds + P1_HIST + par * P1_HSZ);
        const u16* sta = (const u16*)(lds + P1_STA + par * P1_SSZ);
#pragma unroll
        for (int r = 0; r < (NBIN + 1023) / 1024; r++) {
            const int i = t + r * 1024;
            if (i < NBIN) {
                const u32 h = hist[i];
                if (h & 1) *(u32x2*)(lds + P1_ST + ((u32)sta[i] + h) * 8) = u32x2{DUMMY_X, 0u};
            }
        }
        const u32 hn = P1_HIST + (par ^ 1) * P1_HSZ;
#pragma unroll
        for (int g = 0; g < 4; g++) {
            if (g * 4096 < n) {
                const bool v = g * 4096 + (int)t * 4 < n;
                u32 p[4];
                if (v) {
#pragma unroll
                    for (int k = 0; k < 4; k++)
                        p[k] = __hip_atomic_fetch_add(AL4(lds + P1_CUR + (Y[g][k] >> 16)), 8u, __ATOMIC_RELAXED, __HIP_MEMORY_SCOPE_WORKGROUP);
#pragma unroll
                    for (int k = 0; k < 4; k++) *(u32x2*)(lds + P1_ST + p[k]) = u32x2{X[g][k], Y[g][k]};
                }
            }
            SCHED_FENCE();
            if (g * 4096 < nn) {
                p1_keys(raw[g], freq, X[g], Y[g]);
                if (g * 4096 + (int)t * 4 < nn) {
#pragma unroll
                    for (int k = 0; k < 4; k++)
                        __hip_atomic_fetch_add(AL4(lds + hn + (Y[g][k] >> 16)), 1u, __ATOMIC_RELAXED, __HIP_MEMORY_SCOPE_WORKGROUP);
                }
            }
            SCHED_FENCE();
            if (g * 4096 < n2) p1_load(raw[g], g, idx_in, idx_out, angle, base + CS0 + CS1, n2, t);
            SCHED_FENCE();
        }
        LDS_BARRIER();
        if (wave == 0 && nn) p1_scan(lds, par ^ 1, lane);
        {
            const int chunk = blockIdx.x * 3 + j;
            const u32 np = __builtin_amdgcn_readfirstlane((u32)sta[NBIN]);
            auto rr = __builtin_amdgcn_make_buffer_rsrc((void*)(rec + (size_t)chunk * CSTR * 6), 0, np * 6, 0x00020000);
            const u32 cth = nn ? 960u : 1024u, c0 = nn ? t - 64u : t;
#pragma unroll 3
            for (u32 i = c0; i < np / 2 && (wave > 0 || !nn); i += cth) {
                const u32x4 v = *(const u32x4*)(lds + P1_ST + i * 16);
                u32x3 d;
                d[0] = v[0];
                d[1] = __builtin_amdgcn_perm(v[2], v[1], 0x05040100u);
                d[2] = __builtin_amdgcn_perm(v[3], v[2], 0x05040302u);
                __builtin_amdgcn_raw_buffer_store_b96(d, rr, i * 12, 0, 0);
            }
            if (t >= 768) {
                const int ob = t - 768;
                u32* hp = (u32*)(lds + P1_HIST + par * P1_HSZ) + ob;
                u32 e[NPH];
#pragma unroll
                for (int ph = 0; ph < NPH; ph++) {
                    const u32 h = hp[ph * NOB];
                    hp[ph * NOB] = 0;
                    e[ph] = ((u32)sta[ph * NOB + ob] >> 1) | (min(h, LEN_SAT) << 14);
                    if (__builtin_expect(h >= LEN_SAT, 0)) side[((size_t)chunk * NOB + ob) * NPH + ph] = h;
                }
                tab[(size_t)chunk * NOB + ob] = u32x4{e[0] | (e[1] << 25), (e[1] >> 7) | (e[2] << 18), (e[2] >> 14) | (e[3] << 11), (e[3] >> 21) | (e[4] << 4)};
            }
        }
        LDS_BARRIER();
    }
}

constexpr int NT = 8, NB = 8, NITEM = 2 * NB, TPC = 10;
constexpr int CPS = NCH / NITEM;
constexpr int NTILE = (EPB + 15) / 16;
static_assert(NITEM * CPS == NCH && CPS <= 64 && NITEM == 16, "builder chunk sets");
static_assert(NT * TPC >= NTILE, "consumer tiles");
constexpr int CAP = 8448;
constexpr int WIN = 7 * 64;
constexpr int PF = 4;
constexpr int GWIN = PF * 64;
constexpr int WRS = 80;
constexpr int P2_NODES = 0;
constexpr int TRASH_NODE = 2 * CAP + 1;
constexpr int P2_W = ((2 + 2 * CAP) * 8 + 15) / 16 * 16;
constexpr int P2_CNT = P2_W + NT * 16 * WRS;
constexpr int P2_CUR = P2_CNT + 2 * NB * 96 * 4;
constexpr int P2_SEG = P2_CUR + NB * 96 * 4;
constexpr int P2_WTOT = P2_SEG + 3 * 96 * 4;
constexpr int P2_WTOP = P2_WTOT + NPH * 16 * 4;
constexpr int P2_SYNC = P2_WTOP + NPH * 16 * 4;
constexpr int P2_WSCR = P2_SYNC + 32;
constexpr int WSCR_SZ = 256 + 128;
constexpr int P2_LDS = P2_WSCR + NITEM * WSCR_SZ;
static_assert(P2_LDS <= 163840, "LDS");
static_assert(P2_WSCR % 16 == 0 && P2_W % 16 == 0, "align");
constexpr u32 OOB = 0xFFFFFF00u;

__device__ __forceinline__ u32 mbcnt64(u32 lo, u32 hi, u32 base) {
    return __builtin_amdgcn_mbcnt_hi(hi, __builtin_amdgcn_mbcnt_lo(lo, base));
}
__device__ __forceinline__ void lds_wait_ge(char* lds, int off, u32 target) {
    for (int spin = 0; spin < (1 << 20); spin++) {
        u32 v;
        asm volatile("ds_read_b32 %0, %1\n\ts_waitcnt lgkmcnt(0)" : "=v"(v) : "v"((u32)(uintptr_t)(lds + off)) : "memory");
        if ((u32)__builtin_amdgcn_readfirstlane(v) >= target) break;
        __builtin_amdgcn_s_sleep(2);
    }
}
__device__ __forceinline__ void lds_signal(char* lds, int off, int lane) {
    asm volatile("s_waitcnt lgkmcnt(0)" ::: "memory");
    if (lane == 0) __hip_atomic_fetch_add(AL4(lds + off), 1u, __ATOMIC_RELAXED, __HIP_MEMORY_SCOPE_WORKGROUP);
    asm volatile("" ::: "memory");
}

__global__ void __launch_bounds__(1024) p2_acc(const u16* __restrict__ m, const u16* __restrict__ dist, const u16* __restrict__ centers,
                                               const u16* __restrict__ w_rbf, const u16* __restrict__ w_sbf,
                                               const u16* __restrict__ w_down, const u16* __restrict__ w_up,
                                               const char* __restrict__ rec, const u32x4* __restrict__ tab, const u32* __restrict__ side, u16* __restrict__ out) {
    __shared__ __attribute__((aligned(16))) char lds[P2_LDS];
    const u32 t = threadIdx.x;
    const int lane = t & 63;
    const int wave = __builtin_amdgcn_readfirstlane(t >> 6);
    const int ob = (blockIdx.x & 7) * (NOB / 8) + (blockIdx.x >> 3);
    const bool builder = wave >= NT;

    if (builder) {
        const int bw = wave - NT;
        const auto rt = __builtin_amdgcn_make_buffer_rsrc((void*)tab, 0, NCH * NOB * 16, 0x00020000);
        auto unpack = [&](const u32x4& w, u32 c, bool cok, u32 (&e)[NPH]) {
            const u32 M = (1u << 25) - 1;
            const u32 f[NPH] = {w[0] & M, __builtin_amdgcn_alignbit(w[1], w[0], 25) & M, __builtin_amdgcn_alignbit(w[2], w[1], 18) & M,
                                __builtin_amdgcn_alignbit(w[3], w[2], 11) & M, w[3] >> 4};
            u32 sat = 0;
#pragma unroll
            for (int p = 0; p < NPH; p++) { e[p] = ((f[p] & 0x3fffu) << 1) | ((f[p] >> 14) << 16); sat |= (f[p] >> 14) == LEN_SAT; }
            if (__builtin_expect(__any(sat), 0)) {
#pragma unroll
                for (int p = 0; p < NPH; p++)
                    if (cok && (e[p] >> 16) == LEN_SAT) e[p] = (e[p] & 0xffffu) | (side[((size_t)c * NOB + ob) * NPH + p] << 16);
            }
        };
        u32 dl[2][NPH], pf[2][NPH];
        {
            u32 ent[2][NPH];
            u32x4 w[2];
#pragma unroll
            for (int s = 0; s < 2; s++)
                w[s] = __builtin_amdgcn_raw_buffer_load_b128(rt, lane < CPS ? (u32)(((bw * 2 + s) * CPS + lane) * NOB + ob) * 16u : OOB, 0, 0);
#pragma unroll
            for (int s = 0; s < 2; s++) unpack(w[s], (bw * 2 + s) * CPS + lane, lane < CPS, ent[s]);
            SCHED_FENCE();
#pragma unroll
            for (int s = 0; s < 2; s++) {
                const u32 chunkBase = ((bw * 2 + s) * CPS + lane) * (CSTR / 2);
#pragma unroll
                for (int p = 0; p < NPH; p++) {
                    const u32 len = ent[s][p] >> 16, lp = (len + 1) >> 1;
                    const u32 incp = dpp_add_scan(lp);
                    const u32 incr = 2 * incp - __popcll(__ballot(len & 1));
                    if (lane == 63) {
                        *(u32*)(lds + P2_WTOT + (p * 16 + bw * 2 + s) * 4) = incr;
                        *(u32*)(lds + P2_WTOP + (p * 16 + bw * 2 + s) * 4) = incp;
                    }
                    const u32 P = incp - lp;
                    dl[s][p] = chunkBase + ((ent[s][p] & 0xffffu) >> 1) - P;
                    pf[s][p] = lp ? P : (P | 0x80000000u);
                    asm volatile("" : "+v"(dl[s][p]), "+v"(pf[s][p]));
                }
            }
        }
        LDS_BARRIER();
        int totv = 0;
        if (lane < NPH) {
#pragma unroll
            for (int j = 0; j < 4; j++) {
                const u32x4 q4 = *(const u32x4*)(lds + P2_WTOT + (lane * 16 + j * 4) * 4);
                totv += q4[0] + q4[1] + q4[2] + q4[3];
            }
        }
        auto total_of = [&](int p) { return __builtin_amdgcn_readlane(totv, p); };
        auto it_norm = [&](int& p, int& lo) { while (p < NPH && lo >= total_of(p)) { p++; lo = 0; } };
        const auto rrec = __builtin_amdgcn_make_buffer_rsrc((void*)rec, 0, NCH * CSTR * 6, 0x00020000);
        char* const curB = lds + P2_CUR + bw * 96 * 4;
        const u32 ldsb = (u32)(uintptr_t)lds;

        struct Item { u32 Pn; int N; };
        auto gen_issue = [&](const Item& I, u32x2 (&R)[PF], char* wscr, int w0) {
            int qb = w0 + lane;
            asm volatile("" : "+v"(qb));
            if (lane < 16) *(u64*)(wscr + 256 + lane * 8) = 0ull;
            const int pos = (int)I.Pn - 1 - w0;
            const bool has = (int)I.Pn > 0;
            const bool inw = has && pos >= 0 && pos < GWIN;
            const u32 ma = inw ? (u32)(pos >> 6) * 8u : 120u;
            __hip_atomic_fetch_or((u64*)__builtin_assume_aligned(wscr + 256 + ma, 8), 1ull << (pos & 63), __ATOMIC_RELAXED, __HIP_MEMORY_SCOPE_WORKGROUP);
            u32 kb = __popcll(__ballot(has && pos < 0));
            asm volatile("" ::: "memory");
#pragma unroll
            for (int b = 0; b < PF; b++) {
                const u32x2 M = *(const u32x2*)(wscr + 256 + b * 8);
                const u32 k = mbcnt64(M[0], M[1], kb);
                kb += __popc(M[0]) + __popc(M[1]);
                const u32 d = *(const u32*)(wscr + (k & 63) * 4);
                const int q = qb + b * 64;
                R[b] = __builtin_amdgcn_raw_buffer_load_b64(rrec, q < I.N ? (q + d) * 6u : OOB, 0, 0);
            }
            asm volatile("" ::: "memory");
        };
        auto gen_setup = [&](int p, int lo, int s, Item& I) {
            char* wscr = lds + P2_WSCR + (bw * 2 + s) * WSCR_SZ;
            u32 c = lane;
            asm volatile("" : "+v"(c));
            const bool cok = c < CPS;
            c += (bw * 2 + s) * CPS;
            u32 ea[NPH];
            unpack(__builtin_amdgcn_raw_buffer_load_b128(rt, cok ? (c * NOB + ob) * 16u : OOB, 0, 0), c, cok, ea);
            u32 e = ea[0];
#pragma unroll
            for (int q = 1; q < NPH; q++)
                asm volatile("s_cmp_eq_u32 %2, %3\n\ts_cselect_b64 vcc, -1, 0\n\tv_cndmask_b32 %0, %0, %1, vcc"
                             : "+v"(e) : "v"(ea[q]), "s"(p), "n"(q) : "vcc", "scc");
            const u32 wt = lane < 16 ? *(const u32*)(lds + P2_WTOT + (p * 16 + lane) * 4) : 0u;
            const int winc = dpp_add_scan(wt);
            const int Nw = __builtin_amdgcn_readlane(wt, bw * 2 + s);
            const int B = __builtin_amdgcn_readlane(winc, bw * 2 + s) - Nw;
            const int range = min(CAP, total_of(p) - lo);
            const int len = e >> 16;
            const int P = (int)dpp_add_scan(len) - len;
            const int A = B + P - lo;
            const int klo = max(0, -A);
            const int khi = min(len, range - A);
            const bool ne = khi > klo;
            const int ws = min(max(B - lo, 0), range);
            const int we = min(max(B + Nw - lo, 0), range);
            const int Pn = min(max(A, 0), range) - ws;
            I.Pn = ne ? (u32)Pn : 0x80000000u;
            I.N = we - ws;
            const u64 NE = __ballot(ne);
            const u32 k = mbcnt64((u32)NE, (u32)(NE >> 32), 0);
            *(u32*)(wscr + (ne ? k * 4 : 376u)) = c * CSTR + (e & 0xffffu) + klo - Pn;
            asm volatile("" ::: "memory");
        };
        auto gen_pass = [&](const Item& I, int s, char* cnt, bool place) {
            char* wscr = lds + P2_WSCR + (bw * 2 + s) * WSCR_SZ;
            for (int w0 = 0; w0 < I.N; w0 += GWIN) {
                u32x2 R[PF];
                gen_issue(I, R, wscr, w0);
#pragma unroll
                for (int b = 0; b < PF; b++) {
                    const bool v = w0 + b * 64 + lane < I.N;
                    const u32 ta = v ? ((R[b][0] >> 4) & 0x1FCu) : 95u * 4;
                    if (!place) __hip_atomic_fetch_add(AL4(cnt + ta), 1u, __ATOMIC_RELAXED, __HIP_MEMORY_SCOPE_WORKGROUP);
                    else {
                        const u32 a = __hip_atomic_fetch_add(AL4(curB + ta), v ? 8u : 0u, __ATOMIC_RELAXED, __HIP_MEMORY_SCOPE_WORKGROUP);
                        u32x2 nd;
                        nd[0] = (R[b][0] >> 8) & 0xFFFFE0u;
                        nd[1] = __builtin_amdgcn_perm(R[b][1], R[b][0], 0x05040100u);
                        *(u32x2*)(lds + P2_NODES + a) = nd;
                    }
                }
            }
        };

        const u32 nit0 = lane < NPH ? *(const u32*)(lds + P2_WTOP + (lane * 16 + bw * 2) * 4) : 0u;
        const u32 nit1 = lane < NPH ? *(const u32*)(lds + P2_WTOP + (lane * 16 + bw * 2 + 1) * 4) : 0u;
        u32x12 Ra0, Rb0, Ra1, Rb1; u32x4 Rc0, Rc1;
        const u32 trash = TRASH_NODE * 8;
        int p = 0, lo = 0;
        it_norm(p, lo);
        int pcur = 0;
        for (int i = 0; p < NPH; i++) {
            const int par = i & 1;
            while (pcur < p) {
#pragma unroll
                for (int s = 0; s < 2; s++)
#pragma unroll
                    for (int q = 0; q + 1 < NPH; q++) { dl[s][q] = dl[s][q + 1]; pf[s][q] = pf[s][q + 1]; }
                pcur++;
            }
            char* cnt = lds + P2_CNT + (par * NB + bw) * 96 * 4;
            const int n0 = __builtin_amdgcn_readlane(nit0, p), n1 = __builtin_amdgcn_readlane(nit1, p);
            const bool fast = total_of(p) <= CAP && n0 <= WIN && n1 <= WIN;
            const int ns0 = (n0 + 63) >> 6, ns1 = (n1 + 63) >> 6;
            const u64 pm0 = ~0ull >> ((64 - (n0 & 63)) & 63), pm1 = ~0ull >> ((64 - (n1 & 63)) & 63);
            Item it0, it1;
            if (fast) {
                { const u32 pfv = pf[0][0], dlv = dl[0][0], ws = ldsb + P2_WSCR + (bw * 2) * WSCR_SZ, wm = ws + 256; const int nsl = ns0; const u64 pm = pm0;
                asm volatile(
                "v_mov_b32 v126, 0\n\t"
                "v_mov_b32 v127, 0\n\t"
                "v_lshl_add_u32 v122, %[lane], 3, %[wm]\n\t"
                "v_cmp_le_i32 vcc, 0, %[pf]\n\t"
                "s_mov_b64 exec, 0xffff\n\t"
                "ds_write_b64 v122, v[126:127]\n\t"
                "s_mov_b64 exec, -1\n\t"
                "v_mbcnt_lo_u32_b32 v123, vcc_lo, 0\n\t"
                "v_mbcnt_hi_u32_b32 v123, vcc_hi, v123\n\t"
                "v_lshl_add_u32 v123, v123, 2, %[ws]\n\t"
                "v_add_u32 v122, -1, %[pf]\n\t"
                "s_mov_b64 exec, vcc\n\t"
                "ds_write_b32 v123, %[dl]\n\t"
                "s_mov_b64 exec, -1\n\t"
                "v_cmp_lt_i32 vcc, 0, %[pf]\n\t"
                "v_lshlrev_b64 v[120:121], v122, 1\n\t"
                "v_lshrrev_b32 v122, 6, v122\n\t"
                "v_lshl_add_u32 v122, v122, 3, %[wm]\n\t"
                "v_mov_b32 v124, 0\n\t"
                "s_mov_b64 exec, vcc\n\t"
                "ds_or_b64 v122, v[120:121]\n\t"
                "s_mov_b64 exec, -1\n\t"
                "v_mov_b32 v123, %[wm]\n\t"
                "ds_read_b128 v[48:51], v123 offset:0\n\t"
                "ds_read_b128 v[52:55], v123 offset:16\n\t"
                "ds_read_b128 v[56:59], v123 offset:32\n\t"
                "ds_read_b64 v[60:61], v123 offset:48\n\t"
                "s_waitcnt lgkmcnt(3)\n\t"
                "v_mbcnt_lo_u32_b32 v122, v48, v124\n\t"
                "v_bcnt_u32_b32 v124, v48, v124\n\t"
                "v_mbcnt_hi_u32_b32 v122, v49, v122\n\t"
                "v_bcnt_u32_b32 v124, v49, v124\n\t"
                "v_lshl_add_u32 v122, v122, 2, %[ws]\n\t"
                "ds_read_b32 v64, v122\n\t"
                "v_mbcnt_lo_u32_b32 v122, v50, v124\n\t"
                "v_bcnt_u32_b32 v124, v50, v124\n\t"
                "v_mbcnt_hi_u32_b32 v122, v51, v122\n\t"
                "v_bcnt_u32_b32 v124, v51, v124\n\t"
                "v_lshl_add_u32 v122, v122, 2, %[ws]\n\t"
                "ds_read_b32 v68, v122\n\t"
                "s_waitcnt lgkmcnt(4)\n\t"
                "v_mbcnt_lo_u32_b32 v122, v52, v124\n\t"
                "v_bcnt_u32_b32 v124, v52, v124\n\t"
                "v_mbcnt_hi_u32_b32 v122, v53, v122\n\t"
                "v_bcnt_u32_b32 v124, v53, v124\n\t"
                "v_lshl_add_u32 v122, v122, 2, %[ws]\n\t"
                "ds_read_b32 v72, v122\n\t"
                "v_mbcnt_lo_u32_b32 v122, v54, v124\n\t"
                "v_bcnt_u32_b32 v124, v54, v124\n\t"
                "v_mbcnt_hi_u32_b32 v122, v55, v122\n\t"
                "v_bcnt_u32_b32 v124, v55, v124\n\t"
                "v_lshl_add_u32 v122, v122, 2, %[ws]\n\t"
                "ds_read_b32 v76, v122\n\t"
                "s_waitcnt lgkmcnt(5)\n\t"
                "v_mbcnt_lo_u32_b32 v122, v56, v124\n\t"
                "v_bcnt_u32_b32 v124, v56, v124\n\t"
                "v_mbcnt_hi_u32_b32 v122, v57, v122\n\t"
                "v_bcnt_u32_b32 v124, v57, v124\n\t"
                "v_lshl_add_u32 v122, v122, 2, %[ws]\n\t"
                "ds_read_b32 v80, v122\n\t"
                "v_mbcnt_lo_u32_b32 v122, v58, v124\n\t"
                "v_bcnt_u32_b32 v124, v58, v124\n\t"
                "v_mbcnt_hi_u32_b32 v122, v59, v122\n\t"
                "v_bcnt_u32_b32 v124, v59, v124\n\t"
                "v_lshl_add_u32 v122, v122, 2, %[ws]\n\t"
                "ds_read_b32 v84, v122\n\t"
                "s_waitcnt lgkmcnt(6)\n\t"
                "v_mbcnt_lo_u32_b32 v122, v60, v124\n\t"
                "v_bcnt_u32_b32 v124, v60, v124\n\t"
                "v_mbcnt_hi_u32_b32 v122, v61, v122\n\t"
                "v_bcnt_u32_b32 v124, v61, v124\n\t"
                "v_lshl_add_u32 v122, v122, 2, %[ws]\n\t"
                "ds_read_b32 v88, v122\n\t"
                "s_waitcnt lgkmcnt(6)\n\t"
                "v_add_u32 v64, v64, %[lane]\n\t"
                "v_mul_u32_u24 v64, 12, v64\n\t"
                "s_waitcnt lgkmcnt(5)\n\t"
                "v_add_u32 v68, v68, %[lane]\n\t"
                "v_mul_u32_u24 v68, 12, v68\n\t"
                "s_waitcnt lgkmcnt(4)\n\t"
                "v_add_u32 v72, v72, %[lane]\n\t"
                "v_mul_u32_u24 v72, 12, v72\n\t"
                "s_waitcnt lgkmcnt(3)\n\t"
                "v_add_u32 v76, v76, %[lane]\n\t"
                "v_mul_u32_u24 v76, 12, v76\n\t"
                "s_waitcnt lgkmcnt(2)\n\t"
                "v_add_u32 v80, v80, %[lane]\n\t"
                "v_mul_u32_u24 v80, 12, v80\n\t"
                "s_waitcnt lgkmcnt(1)\n\t"
                "v_add_u32 v84, v84, %[lane]\n\t"
                "v_mul_u32_u24 v84, 12, v84\n\t"
                "s_waitcnt lgkmcnt(0)\n\t"
                "v_add_u32 v88, v88, %[lane]\n\t"
                "v_mul_u32_u24 v88, 12, v88\n\t"
                "s_movk_i32 s73, 1536\n\t"
                "s_cmp_eq_u32 %[ns], 0\n\t"
                "s_cbranch_scc1 LE_%=\n\t"
                "s_sub_u32 s72, 7, %[ns]\n\t"
                "s_mul_i32 s72, s72, 12\n\t"
                "s_getpc_b64 s[74:75]\n\t"
                "PLS_%=:\n\t"
                "s_add_u32 s72, s72, LS_%=-PLS_%=\n\t"
                "s_add_u32 s74, s74, s72\n\t"
                "s_addc_u32 s75, s75, 0\n\t"
                "s_mov_b64 exec, %[pm]\n\t"
                "s_setpc_b64 s[74:75]\n\t"
                "LS_%=:\n\t"
                "buffer_load_dwordx3 v[88:90], v88, %[rr], s73 offen offset:3072\n\t"
                "s_mov_b64 exec, -1\n\t"
                "buffer_load_dwordx3 v[84:86], v84, %[rr], 0 offen offset:3840\n\t"
                "s_mov_b64 exec, -1\n\t"
                "buffer_load_dwordx3 v[80:82], v80, %[rr], 0 offen offset:3072\n\t"
                "s_mov_b64 exec, -1\n\t"
                "buffer_load_dwordx3 v[76:78], v76, %[rr], 0 offen offset:2304\n\t"
                "s_mov_b64 exec, -1\n\t"
                "buffer_load_dwordx3 v[72:74], v72, %[rr], 0 offen offset:1536\n\t"
                "s_mov_b64 exec, -1\n\t"
                "buffer_load_dwordx3 v[68:70], v68, %[rr], 0 offen offset:768\n\t"
                "s_mov_b64 exec, -1\n\t"
                "buffer_load_dwordx3 v[64:66], v64, %[rr], 0 offen offset:0\n\t"
                "s_mov_b64 exec, -1\n\t"
                "LE_%=:\n\t"
                "s_nop 0"
                : "={v[64:75]}"(Ra0), "={v[76:87]}"(Rb0), "={v[88:91]}"(Rc0)
                : [pf] "v"(pfv), [dl] "v"(dlv), [lane] "v"(lane), [ws] "s"(ws), [wm] "s"(wm), [ns] "s"(nsl), [pm] "s"(pm), [rr] "s"(rrec)
                : "vcc", "scc", "memory", "v48", "v49", "v50", "v51", "v52", "v53", "v54", "v55", "v56", "v57", "v58", "v59", "v60", "v61", "v120", "v121", "v122", "v123", "v124", "v125", "v126", "v127", "s72", "s73", "s74", "s75");
                }
                { const u32 pfv = pf[1][0], dlv = dl[1][0], ws = ldsb + P2_WSCR + (bw * 2 + 1) * WSCR_SZ, wm = ws + 256; const int nsl = ns1; const u64 pm = pm1;
                asm volatile(
                "v_mov_b32 v126, 0\n\t"
                "v_mov_b32 v127, 0\n\t"
                "v_lshl_add_u32 v122, %[lane], 3, %[wm]\n\t"
                "v_cmp_le_i32 vcc, 0, %[pf]\n\t"
                "s_mov_b64 exec, 0xffff\n\t"
                "ds_write_b64 v122, v[126:127]\n\t"
                "s_mov_b64 exec, -1\n\t"
                "v_mbcnt_lo_u32_b32 v123, vcc_lo, 0\n\t"
                "v_mbcnt_hi_u32_b32 v123, vcc_hi, v123\n\t"
                "v_lshl_add_u32 v123, v123, 2, %[ws]\n\t"
                "v_add_u32 v122, -1, %[pf]\n\t"
                "s_mov_b64 exec, vcc\n\t"
                "ds_write_b32 v123, %[dl]\n\t"
                "s_mov_b64 exec, -1\n\t"
                "v_cmp_lt_i32 vcc, 0, %[pf]\n\t"
                "v_lshlrev_b64 v[120:121], v122, 1\n\t"
                "v_lshrrev_b32 v122, 6, v122\n\t"
                "v_lshl_add_u32 v122, v122, 3, %[wm]\n\t"
                "v_mov_b32 v124, 0\n\t"
                "s_mov_b64 exec, vcc\n\t"
                "ds_or_b64 v122, v[120:121]\n\t"
                "s_mov_b64 exec, -1\n\t"
                "v_mov_b32 v123, %[wm]\n\t"
                "ds_read_b128 v[48:51], v123 offset:0\n\t"
                "ds_read_b128 v[52:55], v123 offset:16\n\t"
                "ds_read_b128 v[56:59], v123 offset:32\n\t"
                "ds_read_b64 v[60:61], v123 offset:48\n\t"
                "s_waitcnt lgkmcnt(3)\n\t"
                "v_mbcnt_lo_u32_b32 v122, v48, v124\n\t"
                "v_bcnt_u32_b32 v124, v48, v124\n\t"
                "v_mbcnt_hi_u32_b32 v122, v49, v122\n\t"
                "v_bcnt_u32_b32 v124, v49, v124\n\t"
                "v_lshl_add_u32 v122, v122, 2, %[ws]\n\t"
                "ds_read_b32 v92, v122\n\t"
                "v_mbcnt_lo_u32_b32 v122, v50, v124\n\t"
                "v_bcnt_u32_b32 v124, v50, v124\n\t"
                "v_mbcnt_hi_u32_b32 v122, v51, v122\n\t"
                "v_bcnt_u32_b32 v124, v51, v124\n\t"
                "v_lshl_add_u32 v122, v122, 2, %[ws]\n\t"
                "ds_read_b32 v96, v122\n\t"
                "s_waitcnt lgkmcnt(4)\n\t"
                "v_mbcnt_lo_u32_b32 v122, v52, v124\n\t"
                "v_bcnt_u32_b32 v124, v52, v124\n\t"
                "v_mbcnt_hi_u32_b32 v122, v53, v122\n\t"
                "v_bcnt_u32_b32 v124, v53, v124\n\t"
                "v_lshl_add_u32 v122, v122, 2, %[ws]\n\t"
                "ds_read_b32 v100, v122\n\t"
                "v_mbcnt_lo_u32_b32 v122, v54, v124\n\t"
                "v_bcnt_u32_b32 v124, v54, v124\n\t"
                "v_mbcnt_hi_u32_b32 v122, v55, v122\n\t"
                "v_bcnt_u32_b32 v124, v55, v124\n\t"
                "v_lshl_add_u32 v122, v122, 2, %[ws]\n\t"
                "ds_read_b32 v104, v122\n\t"
                "s_waitcnt lgkmcnt(5)\n\t"
                "v_mbcnt_lo_u32_b32 v122, v56, v124\n\t"
                "v_bcnt_u32_b32 v124, v56, v124\n\t"
                "v_mbcnt_hi_u32_b32 v122, v57, v122\n\t"
                "v_bcnt_u32_b32 v124, v57, v124\n\t"
                "v_lshl_add_u32 v122, v122, 2, %[ws]\n\t"
                "ds_read_b32 v108, v122\n\t"
                "v_mbcnt_lo_u32_b32 v122, v58, v124\n\t"
                "v_bcnt_u32_b32 v124, v58, v124\n\t"
                "v_mbcnt_hi_u32_b32 v122, v59, v122\n\t"
                "v_bcnt_u32_b32 v124, v59, v124\n\t"
                "v_lshl_add_u32 v122, v122, 2, %[ws]\n\t"
                "ds_read_b32 v112, v122\n\t"
                "s_waitcnt lgkmcnt(6)\n\t"
                "v_mbcnt_lo_u32_b32 v122, v60, v124\n\t"
                "v_bcnt_u32_b32 v124, v60, v124\n\t"
                "v_mbcnt_hi_u32_b32 v122, v61, v122\n\t"
                "v_bcnt_u32_b32 v124, v61, v124\n\t"
                "v_lshl_add_u32 v122, v122, 2, %[ws]\n\t"
                "ds_read_b32 v116, v122\n\t"
                "s_waitcnt lgkmcnt(6)\n\t"
                "v_add_u32 v92, v92, %[lane]\n\t"
                "v_mul_u32_u24 v92, 12, v92\n\t"
                "s_waitcnt lgkmcnt(5)\n\t"
                "v_add_u32 v96, v96, %[lane]\n\t"
                "v_mul_u32_u24 v96, 12, v96\n\t"
                "s_waitcnt lgkmcnt(4)\n\t"
                "v_add_u32 v100, v100, %[lane]\n\t"
                "v_mul_u32_u24 v100, 12, v100\n\t"
                "s_waitcnt lgkmcnt(3)\n\t"
                "v_add_u32 v104, v104, %[lane]\n\t"
                "v_mul_u32_u24 v104, 12, v104\n\t"
                "s_waitcnt lgkmcnt(2)\n\t"
                "v_add_u32 v108, v108, %[lane]\n\t"
                "v_mul_u32_u24 v108, 12, v108\n\t"
                "s_waitcnt lgkmcnt(1)\n\t"
                "v_add_u32 v112, v112, %[lane]\n\t"
                "v_mul_u32_u24 v112, 12, v112\n\t"
                "s_waitcnt lgkmcnt(0)\n\t"
                "v_add_u32 v116, v116, %[lane]\n\t"
                "v_mul_u32_u24 v116, 12, v116\n\t"
                "s_movk_i32 s73, 1536\n\t"
                "s_cmp_eq_u32 %[ns], 0\n\t"
                "s_cbranch_scc1 LE_%=\n\t"
                "s_sub_u32 s72, 7, %[ns]\n\t"
                "s_mul_i32 s72, s72, 12\n\t"
                "s_getpc_b64 s[74:75]\n\t"
                "PLS_%=:\n\t"
                "s_add_u32 s72, s72, LS_%=-PLS_%=\n\t"
                "s_add_u32 s74, s74, s72\n\t"
                "s_addc_u32 s75, s75, 0\n\t"
                "s_mov_b64 exec, %[pm]\n\t"
                "s_setpc_b64 s[74:75]\n\t"
                "LS_%=:\n\t"
                "buffer_load_dwordx3 v[116:118], v116, %[rr], s73 offen offset:3072\n\t"
                "s_mov_b64 exec, -1\n\t"
                "buffer_load_dwordx3 v[112:114], v112, %[rr], 0 offen offset:3840\n\t"
                "s_mov_b64 exec, -1\n\t"
                "buffer_load_dwordx3 v[108:110], v108, %[rr], 0 offen offset:3072\n\t"
                "s_mov_b64 exec, -1\n\t"
                "buffer_load_dwordx3 v[104:106], v104, %[rr], 0 offen offset:2304\n\t"
                "s_mov_b64 exec, -1\n\t"
                "buffer_load_dwordx3 v[100:102], v100, %[rr], 0 offen offset:1536\n\t"
                "s_mov_b64 exec, -1\n\t"
                "buffer_load_dwordx3 v[96:98], v96, %[rr], 0 offen offset:768\n\t"
                "s_mov_b64 exec, -1\n\t"
                "buffer_load_dwordx3 v[92:94], v92, %[rr], 0 offen offset:0\n\t"
                "s_mov_b64 exec, -1\n\t"
                "LE_%=:\n\t"
                "s_nop 0"
                : "={v[92:103]}"(Ra1), "={v[104:115]}"(Rb1), "={v[116:119]}"(Rc1)
                : [pf] "v"(pfv), [dl] "v"(dlv), [lane] "v"(lane), [ws] "s"(ws), [wm] "s"(wm), [ns] "s"(nsl), [pm] "s"(pm), [rr] "s"(rrec)
                : "vcc", "scc", "memory", "v48", "v49", "v50", "v51", "v52", "v53", "v54", "v55", "v56", "v57", "v58", "v59", "v60", "v61", "v120", "v121", "v122", "v123", "v124", "v125", "v126", "v127", "s72", "s73", "s74", "s75");
                }
                const u32 cb = (u32)(uintptr_t)cnt;
                { const int nsl = ns0; const u64 pm = pm0;
                asm volatile(
                "v_mov_b32 v126, 1\n\t"
                "s_waitcnt vmcnt(0)\n\t"
                "s_cmp_eq_u32 %[ns], 0\n\t"
                "s_cbranch_scc1 LE_%=\n\t"
                "s_sub_u32 s72, 7, %[ns]\n\t"
                "s_mul_i32 s72, s72, 52\n\t"
                "s_getpc_b64 s[74:75]\n\t"
                "PLS_%=:\n\t"
                "s_add_u32 s72, s72, LS_%=-PLS_%=\n\t"
                "s_add_u32 s74, s74, s72\n\t"
                "s_addc_u32 s75, s75, 0\n\t"
                "s_mov_b64 exec, %[pm]\n\t"
                "s_setpc_b64 s[74:75]\n\t"
                "LS_%=:\n\t"
                "v_bfe_u32 v120, v88, 6, 7\n\t"
                "v_bfe_u32 v121, v89, 22, 7\n\t"
                "v_lshl_add_u32 v120, v120, 2, %[cb]\n\t"
                "v_lshl_add_u32 v121, v121, 2, %[cb]\n\t"
                "ds_add_u32 v120, v126\n\t"
                "ds_add_u32 v121, v126\n\t"
                "s_mov_b64 exec, -1\n\t"
                "v_bfe_u32 v120, v84, 6, 7\n\t"
                "v_bfe_u32 v121, v85, 22, 7\n\t"
                "v_lshl_add_u32 v120, v120, 2, %[cb]\n\t"
                "v_lshl_add_u32 v121, v121, 2, %[cb]\n\t"
                "ds_add_u32 v120, v126\n\t"
                "ds_add_u32 v121, v126\n\t"
                "s_mov_b64 exec, -1\n\t"
                "v_bfe_u32 v120, v80, 6, 7\n\t"
                "v_bfe_u32 v121, v81, 22, 7\n\t"
                "v_lshl_add_u32 v120, v120, 2, %[cb]\n\t"
                "v_lshl_add_u32 v121, v121, 2, %[cb]\n\t"
                "ds_add_u32 v120, v126\n\t"
                "ds_add_u32 v121, v126\n\t"
                "s_mov_b64 exec, -1\n\t"
                "v_bfe_u32 v120, v76, 6, 7\n\t"
                "v_bfe_u32 v121, v77, 22, 7\n\t"
                "v_lshl_add_u32 v120, v120, 2, %[cb]\n\t"
                "v_lshl_add_u32 v121, v121, 2, %[cb]\n\t"
                "ds_add_u32 v120, v126\n\t"
                "ds_add_u32 v121, v126\n\t"
                "s_mov_b64 exec, -1\n\t"
                "v_bfe_u32 v120, v72, 6, 7\n\t"
                "v_bfe_u32 v121, v73, 22, 7\n\t"
                "v_lshl_add_u32 v120, v120, 2, %[cb]\n\t"
                "v_lshl_add_u32 v121, v121, 2, %[cb]\n\t"
                "ds_add_u32 v120, v126\n\t"
                "ds_add_u32 v121, v126\n\t"
                "s_mov_b64 exec, -1\n\t"
                "v_bfe_u32 v120, v68, 6, 7\n\t"
                "v_bfe_u32 v121, v69, 22, 7\n\t"
                "v_lshl_add_u32 v120, v120, 2, %[cb]\n\t"
                "v_lshl_add_u32 v121, v121, 2, %[cb]\n\t"
                "ds_add_u32 v120, v126\n\t"
                "ds_add_u32 v121, v126\n\t"
                "s_mov_b64 exec, -1\n\t"
                "v_bfe_u32 v120, v64, 6, 7\n\t"
                "v_bfe_u32 v121, v65, 22, 7\n\t"
                "v_lshl_add_u32 v120, v120, 2, %[cb]\n\t"
                "v_lshl_add_u32 v121, v121, 2, %[cb]\n\t"
                "ds_add_u32 v120, v126\n\t"
                "ds_add_u32 v121, v126\n\t"
                "s_mov_b64 exec, -1\n\t"
                "LE_%=:\n\t"
                "s_nop 0"
                : "+{v[64:75]}"(Ra0), "+{v[76:87]}"(Rb0), "+{v[88:91]}"(Rc0)
                : [cb] "s"(cb), [ns] "s"(nsl), [pm] "s"(pm)
                : "vcc", "scc", "memory", "v120", "v121", "v126", "s72", "s73", "s74", "s75");
                }
                { const int nsl = ns1; const u64 pm = pm1;
                asm volatile(
                "v_mov_b32 v126, 1\n\t"
                "s_waitcnt vmcnt(0)\n\t"
                "s_cmp_eq_u32 %[ns], 0\n\t"
                "s_cbranch_scc1 LE_%=\n\t"
                "s_sub_u32 s72, 7, %[ns]\n\t"
                "s_mul_i32 s72, s72, 52\n\t"
                "s_getpc_b64 s[74:75]\n\t"
                "PLS_%=:\n\t"
                "s_add_u32 s72, s72, LS_%=-PLS_%=\n\t"
                "s_add_u32 s74, s74, s72\n\t"
                "s_addc_u32 s75, s75, 0\n\t"
                "s_mov_b64 exec, %[pm]\n\t"
                "s_setpc_b64 s[74:75]\n\t"
                "LS_%=:\n\t"
                "v_bfe_u32 v120, v116, 6, 7\n\t"
                "v_bfe_u32 v121, v117, 22, 7\n\t"
                "v_lshl_add_u32 v120, v120, 2, %[cb]\n\t"
                "v_lshl_add_u32 v121, v121, 2, %[cb]\n\t"
                "ds_add_u32 v120, v126\n\t"
                "ds_add_u32 v121, v126\n\t"
                "s_mov_b64 exec, -1\n\t"
                "v_bfe_u32 v120, v112, 6, 7\n\t"
                "v_bfe_u32 v121, v113, 22, 7\n\t"
                "v_lshl_add_u32 v120, v120, 2, %[cb]\n\t"
                "v_lshl_add_u32 v121, v121, 2, %[cb]\n\t"
                "ds_add_u32 v120, v126\n\t"
                "ds_add_u32 v121, v126\n\t"
                "s_mov_b64 exec, -1\n\t"
                "v_bfe_u32 v120, v108, 6, 7\n\t"
                "v_bfe_u32 v121, v109, 22, 7\n\t"
                "v_lshl_add_u32 v120, v120, 2, %[cb]\n\t"
                "v_lshl_add_u32 v121, v121, 2, %[cb]\n\t"
                "ds_add_u32 v120, v126\n\t"
                "ds_add_u32 v121, v126\n\t"
                "s_mov_b64 exec, -1\n\t"
                "v_bfe_u32 v120, v104, 6, 7\n\t"
                "v_bfe_u32 v121, v105, 22, 7\n\t"
                "v_lshl_add_u32 v120, v120, 2, %[cb]\n\t"
                "v_lshl_add_u32 v121, v121, 2, %[cb]\n\t"
                "ds_add_u32 v120, v126\n\t"
                "ds_add_u32 v121, v126\n\t"
                "s_mov_b64 exec, -1\n\t"
                "v_bfe_u32 v120, v100, 6, 7\n\t"
                "v_bfe_u32 v121, v101, 22, 7\n\t"
                "v_lshl_add_u32 v120, v120, 2, %[cb]\n\t"
                "v_lshl_add_u32 v121, v121, 2, %[cb]\n\t"
                "ds_add_u32 v120, v126\n\t"
                "ds_add_u32 v121, v126\n\t"
                "s_mov_b64 exec, -1\n\t"
                "v_bfe_u32 v120, v96, 6, 7\n\t"
                "v_bfe_u32 v121, v97, 22, 7\n\t"
                "v_lshl_add_u32 v120, v120, 2, %[cb]\n\t"
                "v_lshl_add_u32 v121, v121, 2, %[cb]\n\t"
                "ds_add_u32 v120, v126\n\t"
                "ds_add_u32 v121, v126\n\t"
                "s_mov_b64 exec, -1\n\t"
                "v_bfe_u32 v120, v92, 6, 7\n\t"
                "v_bfe_u32 v121, v93, 22, 7\n\t"
                "v_lshl_add_u32 v120, v120, 2, %[cb]\n\t"
                "v_lshl_add_u32 v121, v121, 2, %[cb]\n\t"
                "ds_add_u32 v120, v126\n\t"
                "ds_add_u32 v121, v126\n\t"
                "s_mov_b64 exec, -1\n\t"
                "LE_%=:\n\t"
                "s_nop 0"
                : "+{v[92:103]}"(Ra1), "+{v[104:115]}"(Rb1), "+{v[116:119]}"(Rc1)
                : [cb] "s"(cb), [ns] "s"(nsl), [pm] "s"(pm)
                : "vcc", "scc", "memory", "v120", "v121", "v126", "s72", "s73", "s74", "s75");
                }
            } else {
                gen_setup(p, lo, 0, it0);
                gen_setup(p, lo, 1, it1);
                gen_pass(it0, 0, cnt, false);
                gen_pass(it1, 1, cnt, false);
            }
            lds_signal(lds, P2_SYNC + 16, lane);
            lds_wait_ge(lds, P2_SYNC + 16, NB * (i + 1));
            {
                char* cz = lds + P2_CNT + ((par ^ 1) * NB + bw) * 96 * 4;
                *(u32*)(cz + lane * 4) = 0u;
                if (lane < 32) *(u32*)(cz + 256 + lane * 4) = 0u;
            }
            {
                const char* cb = lds + P2_CNT + par * NB * 96 * 4;
                u32 tot0 = 0, tot1 = 0, my0 = 0, my1 = 0;
#pragma unroll
                for (int b = 0; b < NB; b++) {
                    const u32 c0 = *(const u32*)(cb + (b * 96 + lane) * 4);
                    const u32 c1 = *(const u32*)(cb + (b * 96 + 64 + (lane & 15)) * 4);
                    if (b < bw) { my0 += c0; my1 += c1; }
                    tot0 += c0; tot1 += c1;
                }
                if (lane >= 15) { tot1 = 0; my1 = 0; }
                const u32 inc0 = dpp_add_scan(tot0);
                const u32 sum0 = __builtin_amdgcn_readlane(inc0, 63);
                const u32 inc1 = dpp_add_scan(tot1) + sum0;
                const u32 base = 1 + par * CAP;
                const u32 s0 = base + inc0 - tot0, s1 = base + inc1 - tot1;
                *(u32*)(curB + lane * 4) = (s0 + my0) * 8;
                if (lane < 32) *(u32*)(curB + 256 + lane * 4) = lane < 15 ? (s1 + my1) * 8 : TRASH_NODE * 8;
                if (bw == 0) {
                    char* sg = lds + P2_SEG + (i % 3) * 96 * 4;
                    *(u32*)(sg + lane * 4) = s0;
                    if (lane < 32) *(u32*)(sg + 256 + lane * 4) = s1;
                }
            }
            if (i >= 2) lds_wait_ge(lds, P2_SYNC + 8 + par * 4, NT * (i >> 1));
            if (fast) {
                asm volatile("s_waitcnt lgkmcnt(0)" ::: "memory");
                const u32 cb = (u32)(uintptr_t)curB;
                { const int nsl = ns0; const u64 pm = pm0;
                asm volatile(
                "v_mov_b32 v126, 8\n\t"
                "s_mov_b32 s76, 0x05040100\n\t"
                "s_mov_b32 s77, 0xffffe0\n\t"
                "s_mov_b32 s78, 0x07060302\n\t"
                "s_cmp_eq_u32 %[ns], 0\n\t"
                "s_cbranch_scc1 LE_%=\n\t"
                "s_sub_u32 s72, 7, %[ns]\n\t"
                "s_mul_i32 s72, s72, 52\n\t"
                "s_getpc_b64 s[74:75]\n\t"
                "PLA_%=:\n\t"
                "s_add_u32 s72, s72, LA_%=-PLA_%=\n\t"
                "s_add_u32 s74, s74, s72\n\t"
                "s_addc_u32 s75, s75, 0\n\t"
                "s_mov_b64 exec, %[pm]\n\t"
                "s_setpc_b64 s[74:75]\n\t"
                "LA_%=:\n\t"
                "v_bfe_u32 v120, v88, 6, 7\n\t"
                "v_bfe_u32 v121, v89, 22, 7\n\t"
                "v_lshl_add_u32 v120, v120, 2, %[cb]\n\t"
                "v_lshl_add_u32 v121, v121, 2, %[cb]\n\t"
                "ds_add_rtn_u32 v60, v120, v126\n\t"
                "ds_add_rtn_u32 v61, v121, v126\n\t"
                "s_mov_b64 exec, -1\n\t"
                "v_bfe_u32 v120, v84, 6, 7\n\t"
                "v_bfe_u32 v121, v85, 22, 7\n\t"
                "v_lshl_add_u32 v120, v120, 2, %[cb]\n\t"
                "v_lshl_add_u32 v121, v121, 2, %[cb]\n\t"
                "ds_add_rtn_u32 v58, v120, v126\n\t"
                "ds_add_rtn_u32 v59, v121, v126\n\t"
                "s_mov_b64 exec, -1\n\t"
                "v_bfe_u32 v120, v80, 6, 7\n\t"
                "v_bfe_u32 v121, v81, 22, 7\n\t"
                "v_lshl_add_u32 v120, v120, 2, %[cb]\n\t"
                "v_lshl_add_u32 v121, v121, 2, %[cb]\n\t"
                "ds_add_rtn_u32 v56, v120, v126\n\t"
                "ds_add_rtn_u32 v57, v121, v126\n\t"
                "s_mov_b64 exec, -1\n\t"
                "v_bfe_u32 v120, v76, 6, 7\n\t"
                "v_bfe_u32 v121, v77, 22, 7\n\t"
                "v_lshl_add_u32 v120, v120, 2, %[cb]\n\t"
                "v_lshl_add_u32 v121, v121, 2, %[cb]\n\t"
                "ds_add_rtn_u32 v54, v120, v126\n\t"
                "ds_add_rtn_u32 v55, v121, v126\n\t"
                "s_mov_b64 exec, -1\n\t"
                "v_bfe_u32 v120, v72, 6, 7\n\t"
                "v_bfe_u32 v121, v73, 22, 7\n\t"
                "v_lshl_add_u32 v120, v120, 2, %[cb]\n\t"
                "v_lshl_add_u32 v121, v121, 2, %[cb]\n\t"
                "ds_add_rtn_u32 v52, v120, v126\n\t"
                "ds_add_rtn_u32 v53, v121, v126\n\t"
                "s_mov_b64 exec, -1\n\t"
                "v_bfe_u32 v120, v68, 6, 7\n\t"
                "v_bfe_u32 v121, v69, 22, 7\n\t"
                "v_lshl_add_u32 v120, v120, 2, %[cb]\n\t"
                "v_lshl_add_u32 v121, v121, 2, %[cb]\n\t"
                "ds_add_rtn_u32 v50, v120, v126\n\t"
                "ds_add_rtn_u32 v51, v121, v126\n\t"
                "s_mov_b64 exec, -1\n\t"
                "v_bfe_u32 v120, v64, 6, 7\n\t"
                "v_bfe_u32 v121, v65, 22, 7\n\t"
                "v_lshl_add_u32 v120, v120, 2, %[cb]\n\t"
                "v_lshl_add_u32 v121, v121, 2, %[cb]\n\t"
                "ds_add_rtn_u32 v48, v120, v126\n\t"
                "ds_add_rtn_u32 v49, v121, v126\n\t"
                "s_mov_b64 exec, -1\n\t"
                "v_perm_b32 v67, v66, v65, s78\n\t"
                "v_alignbit_b32 v66, v66, v65, 24\n\t"
                "v_perm_b32 v65, v65, v64, s76\n\t"
                "v_lshrrev_b32 v64, 8, v64\n\t"
                "v_and_b32 v66, s77, v66\n\t"
                "v_and_b32 v64, s77, v64\n\t"
                "v_perm_b32 v71, v70, v69, s78\n\t"
                "v_alignbit_b32 v70, v70, v69, 24\n\t"
                "v_perm_b32 v69, v69, v68, s76\n\t"
                "v_lshrrev_b32 v68, 8, v68\n\t"
                "v_and_b32 v70, s77, v70\n\t"
                "v_and_b32 v68, s77, v68\n\t"
                "v_perm_b32 v75, v74, v73, s78\n\t"
                "v_alignbit_b32 v74, v74, v73, 24\n\t"
                "v_perm_b32 v73, v73, v72, s76\n\t"
                "v_lshrrev_b32 v72, 8, v72\n\t"
                "v_and_b32 v74, s77, v74\n\t"
                "v_and_b32 v72, s77, v72\n\t"
                "v_perm_b32 v79, v78, v77, s78\n\t"
                "v_alignbit_b32 v78, v78, v77, 24\n\t"
                "v_perm_b32 v77, v77, v76, s76\n\t"
                "v_lshrrev_b32 v76, 8, v76\n\t"
                "v_and_b32 v78, s77, v78\n\t"
                "v_and_b32 v76, s77, v76\n\t"
                "v_perm_b32 v83, v82, v81, s78\n\t"
                "v_alignbit_b32 v82, v82, v81, 24\n\t"
                "v_perm_b32 v81, v81, v80, s76\n\t"
                "v_lshrrev_b32 v80, 8, v80\n\t"
                "v_and_b32 v82, s77, v82\n\t"
                "v_and_b32 v80, s77, v80\n\t"
                "v_perm_b32 v87, v86, v85, s78\n\t"
                "v_alignbit_b32 v86, v86, v85, 24\n\t"
                "v_perm_b32 v85, v85, v84, s76\n\t"
                "v_lshrrev_b32 v84, 8, v84\n\t"
                "v_and_b32 v86, s77, v86\n\t"
                "v_and_b32 v84, s77, v84\n\t"
                "v_perm_b32 v91, v90, v89, s78\n\t"
                "v_alignbit_b32 v90, v90, v89, 24\n\t"
                "v_perm_b32 v89, v89, v88, s76\n\t"
                "v_lshrrev_b32 v88, 8, v88\n\t"
                "v_and_b32 v90, s77, v90\n\t"
                "v_and_b32 v88, s77, v88\n\t"
                "s_waitcnt lgkmcnt(0)\n\t"
                "v_min_u32 v48, %[tr], v48\n\t"
                "v_min_u32 v49, %[tr], v49\n\t"
                "v_min_u32 v50, %[tr], v50\n\t"
                "v_min_u32 v51, %[tr], v51\n\t"
                "v_min_u32 v52, %[tr], v52\n\t"
                "v_min_u32 v53, %[tr], v53\n\t"
                "v_min_u32 v54, %[tr], v54\n\t"
                "v_min_u32 v55, %[tr], v55\n\t"
                "v_min_u32 v56, %[tr], v56\n\t"
                "v_min_u32 v57, %[tr], v57\n\t"
                "v_min_u32 v58, %[tr], v58\n\t"
                "v_min_u32 v59, %[tr], v59\n\t"
                "v_min_u32 v60, %[tr], v60\n\t"
                "v_min_u32 v61, %[tr], v61\n\t"
                "s_cmp_eq_u32 %[ns], 0\n\t"
                "s_cbranch_scc1 LE_%=\n\t"
                "s_sub_u32 s72, 7, %[ns]\n\t"
                "s_mul_i32 s72, s72, 20\n\t"
                "s_getpc_b64 s[74:75]\n\t"
                "PLB_%=:\n\t"
                "s_add_u32 s72, s72, LB_%=-PLB_%=\n\t"
                "s_add_u32 s74, s74, s72\n\t"
                "s_addc_u32 s75, s75, 0\n\t"
                "s_mov_b64 exec, %[pm]\n\t"
                "s_setpc_b64 s[74:75]\n\t"
                "LB_%=:\n\t"
                "ds_write_b64 v60, v[88:89]\n\t"
                "ds_write_b64 v61, v[90:91]\n\t"
                "s_mov_b64 exec, -1\n\t"
                "ds_write_b64 v58, v[84:85]\n\t"
                "ds_write_b64 v59, v[86:87]\n\t"
                "s_mov_b64 exec, -1\n\t"
                "ds_write_b64 v56, v[80:81]\n\t"
                "ds_write_b64 v57, v[82:83]\n\t"
                "s_mov_b64 exec, -1\n\t"
                "ds_write_b64 v54, v[76:77]\n\t"
                "ds_write_b64 v55, v[78:79]\n\t"
                "s_mov_b64 exec, -1\n\t"
                "ds_write_b64 v52, v[72:73]\n\t"
                "ds_write_b64 v53, v[74:75]\n\t"
                "s_mov_b64 exec, -1\n\t"
                "ds_write_b64 v50, v[68:69]\n\t"
                "ds_write_b64 v51, v[70:71]\n\t"
                "s_mov_b64 exec, -1\n\t"
                "ds_write_b64 v48, v[64:65]\n\t"
                "ds_write_b64 v49, v[66:67]\n\t"
                "s_mov_b64 exec, -1\n\t"
                "LE_%=:\n\t"
                "s_nop 0"
                : "+{v[64:75]}"(Ra0), "+{v[76:87]}"(Rb0), "+{v[88:91]}"(Rc0)
                : [cb] "s"(cb), [ns] "s"(nsl), [pm] "s"(pm), [tr] "s"(trash)
                : "vcc", "scc", "memory", "v48", "v49", "v50", "v51", "v52", "v53", "v54", "v55", "v56", "v57", "v58", "v59", "v60", "v61", "v120", "v121", "v126", "s72", "s73", "s74", "s75", "s76", "s77", "s78");
                }
                { const int nsl = ns1; const u64 pm = pm1;
                asm volatile(
                "v_mov_b32 v126, 8\n\t"
                "s_mov_b32 s76, 0x05040100\n\t"
                "s_mov_b32 s77, 0xffffe0\n\t"
                "s_mov_b32 s78, 0x07060302\n\t"
                "s_cmp_eq_u32 %[ns], 0\n\t"
                "s_cbranch_scc1 LE_%=\n\t"
                "s_sub_u32 s72, 7, %[ns]\n\t"
                "s_mul_i32 s72, s72, 52\n\t"
                "s_getpc_b64 s[74:75]\n\t"
                "PLA_%=:\n\t"
                "s_add_u32 s72, s72, LA_%=-PLA_%=\n\t"
                "s_add_u32 s74, s74, s72\n\t"
                "s_addc_u32 s75, s75, 0\n\t"
                "s_mov_b64 exec, %[pm]\n\t"
                "s_setpc_b64 s[74:75]\n\t"
                "LA_%=:\n\t"
                "v_bfe_u32 v120, v116, 6, 7\n\t"
                "v_bfe_u32 v121, v117, 22, 7\n\t"
                "v_lshl_add_u32 v120, v120, 2, %[cb]\n\t"
                "v_lshl_add_u32 v121, v121, 2, %[cb]\n\t"
                "ds_add_rtn_u32 v60, v120, v126\n\t"
                "ds_add_rtn_u32 v61, v121, v126\n\t"
                "s_mov_b64 exec, -1\n\t"
                "v_bfe_u32 v120, v112, 6, 7\n\t"
                "v_bfe_u32 v121, v113, 22, 7\n\t"
                "v_lshl_add_u32 v120, v120, 2, %[cb]\n\t"
                "v_lshl_add_u32 v121, v121, 2, %[cb]\n\t"
                "ds_add_rtn_u32 v58, v120, v126\n\t"
                "ds_add_rtn_u32 v59, v121, v126\n\t"
                "s_mov_b64 exec, -1\n\t"
                "v_bfe_u32 v120, v108, 6, 7\n\t"
                "v_bfe_u32 v121, v109, 22, 7\n\t"
                "v_lshl_add_u32 v120, v120, 2, %[cb]\n\t"
                "v_lshl_add_u32 v121, v121, 2, %[cb]\n\t"
                "ds_add_rtn_u32 v56, v120, v126\n\t"
                "ds_add_rtn_u32 v57, v121, v126\n\t"
                "s_mov_b64 exec, -1\n\t"
                "v_bfe_u32 v120, v104, 6, 7\n\t"
                "v_bfe_u32 v121, v105, 22, 7\n\t"
                "v_lshl_add_u32 v120, v120, 2, %[cb]\n\t"
                "v_lshl_add_u32 v121, v121, 2, %[cb]\n\t"
                "ds_add_rtn_u32 v54, v120, v126\n\t"
                "ds_add_rtn_u32 v55, v121, v126\n\t"
                "s_mov_b64 exec, -1\n\t"
                "v_bfe_u32 v120, v100, 6, 7\n\t"
                "v_bfe_u32 v121, v101, 22, 7\n\t"
                "v_lshl_add_u32 v120, v120, 2, %[cb]\n\t"
                "v_lshl_add_u32 v121, v121, 2, %[cb]\n\t"
                "ds_add_rtn_u32 v52, v120, v126\n\t"
                "ds_add_rtn_u32 v53, v121, v126\n\t"
                "s_mov_b64 exec, -1\n\t"
                "v_bfe_u32 v120, v96, 6, 7\n\t"
                "v_bfe_u32 v121, v97, 22, 7\n\t"
                "v_lshl_add_u32 v120, v120, 2, %[cb]\n\t"
                "v_lshl_add_u32 v121, v121, 2, %[cb]\n\t"
                "ds_add_rtn_u32 v50, v120, v126\n\t"
                "ds_add_rtn_u32 v51, v121, v126\n\t"
                "s_mov_b64 exec, -1\n\t"
                "v_bfe_u32 v120, v92, 6, 7\n\t"
                "v_bfe_u32 v121, v93, 22, 7\n\t"
                "v_lshl_add_u32 v120, v120, 2, %[cb]\n\t"
                "v_lshl_add_u32 v121, v121, 2, %[cb]\n\t"
                "ds_add_rtn_u32 v48, v120, v126\n\t"
                "ds_add_rtn_u32 v49, v121, v126\n\t"
                "s_mov_b64 exec, -1\n\t"
                "v_perm_b32 v95, v94, v93, s78\n\t"
                "v_alignbit_b32 v94, v94, v93, 24\n\t"
                "v_perm_b32 v93, v93, v92, s76\n\t"
                "v_lshrrev_b32 v92, 8, v92\n\t"
                "v_and_b32 v94, s77, v94\n\t"
                "v_and_b32 v92, s77, v92\n\t"
                "v_perm_b32 v99, v98, v97, s78\n\t"
                "v_alignbit_b32 v98, v98, v97, 24\n\t"
                "v_perm_b32 v97, v97, v96, s76\n\t"
                "v_lshrrev_b32 v96, 8, v96\n\t"
                "v_and_b32 v98, s77, v98\n\t"
                "v_and_b32 v96, s77, v96\n\t"
                "v_perm_b32 v103, v102, v101, s78\n\t"
                "v_alignbit_b32 v102, v102, v101, 24\n\t"
                "v_perm_b32 v101, v101, v100, s76\n\t"
                "v_lshrrev_b32 v100, 8, v100\n\t"
                "v_and_b32 v102, s77, v102\n\t"
                "v_and_b32 v100, s77, v100\n\t"
                "v_perm_b32 v107, v106, v105, s78\n\t"
                "v_alignbit_b32 v106, v106, v105, 24\n\t"
                "v_perm_b32 v105, v105, v104, s76\n\t"
                "v_lshrrev_b32 v104, 8, v104\n\t"
                "v_and_b32 v106, s77, v106\n\t"
                "v_and_b32 v104, s77, v104\n\t"
                "v_perm_b32 v111, v110, v109, s78\n\t"
                "v_alignbit_b32 v110, v110, v109, 24\n\t"
                "v_perm_b32 v109, v109, v108, s76\n\t"
                "v_lshrrev_b32 v108, 8, v108\n\t"
                "v_and_b32 v110, s77, v110\n\t"
                "v_and_b32 v108, s77, v108\n\t"
                "v_perm_b32 v115, v114, v113, s78\n\t"
                "v_alignbit_b32 v114, v114, v113, 24\n\t"
                "v_perm_b32 v113, v113, v112, s76\n\t"
                "v_lshrrev_b32 v112, 8, v112\n\t"
                "v_and_b32 v114, s77, v114\n\t"
                "v_and_b32 v112, s77, v112\n\t"
                "v_perm_b32 v119, v118, v117, s78\n\t"
                "v_alignbit_b32 v118, v118, v117, 24\n\t"
                "v_perm_b32 v117, v117, v116, s76\n\t"
                "v_lshrrev_b32 v116, 8, v116\n\t"
                "v_and_b32 v118, s77, v118\n\t"
                "v_and_b32 v116, s77, v116\n\t"
                "s_waitcnt lgkmcnt(0)\n\t"
                "v_min_u32 v48, %[tr], v48\n\t"
                "v_min_u32 v49, %[tr], v49\n\t"
                "v_min_u32 v50, %[tr], v50\n\t"
                "v_min_u32 v51, %[tr], v51\n\t"
                "v_min_u32 v52, %[tr], v52\n\t"
                "v_min_u32 v53, %[tr], v53\n\t"
                "v_min_u32 v54, %[tr], v54\n\t"
                "v_min_u32 v55, %[tr], v55\n\t"
                "v_min_u32 v56, %[tr], v56\n\t"
                "v_min_u32 v57, %[tr], v57\n\t"
                "v_min_u32 v58, %[tr], v58\n\t"
                "v_min_u32 v59, %[tr], v59\n\t"
                "v_min_u32 v60, %[tr], v60\n\t"
                "v_min_u32 v61, %[tr], v61\n\t"
                "s_cmp_eq_u32 %[ns], 0\n\t"
                "s_cbranch_scc1 LE_%=\n\t"
                "s_sub_u32 s72, 7, %[ns]\n\t"
                "s_mul_i32 s72, s72, 20\n\t"
                "s_getpc_b64 s[74:75]\n\t"
                "PLB_%=:\n\t"
                "s_add_u32 s72, s72, LB_%=-PLB_%=\n\t"
                "s_add_u32 s74, s74, s72\n\t"
                "s_addc_u32 s75, s75, 0\n\t"
                "s_mov_b64 exec, %[pm]\n\t"
                "s_setpc_b64 s[74:75]\n\t"
                "LB_%=:\n\t"
                "ds_write_b64 v60, v[116:117]\n\t"
                "ds_write_b64 v61, v[118:119]\n\t"
                "s_mov_b64 exec, -1\n\t"
                "ds_write_b64 v58, v[112:113]\n\t"
                "ds_write_b64 v59, v[114:115]\n\t"
                "s_mov_b64 exec, -1\n\t"
                "ds_write_b64 v56, v[108:109]\n\t"
                "ds_write_b64 v57, v[110:111]\n\t"
                "s_mov_b64 exec, -1\n\t"
                "ds_write_b64 v54, v[104:105]\n\t"
                "ds_write_b64 v55, v[106:107]\n\t"
                "s_mov_b64 exec, -1\n\t"
                "ds_write_b64 v52, v[100:101]\n\t"
                "ds_write_b64 v53, v[102:103]\n\t"
                "s_mov_b64 exec, -1\n\t"
                "ds_write_b64 v50, v[96:97]\n\t"
                "ds_write_b64 v51, v[98:99]\n\t"
                "s_mov_b64 exec, -1\n\t"
                "ds_write_b64 v48, v[92:93]\n\t"
                "ds_write_b64 v49, v[94:95]\n\t"
                "s_mov_b64 exec, -1\n\t"
                "LE_%=:\n\t"
                "s_nop 0"
                : "+{v[92:103]}"(Ra1), "+{v[104:115]}"(Rb1), "+{v[116:119]}"(Rc1)
                : [cb] "s"(cb), [ns] "s"(nsl), [pm] "s"(pm), [tr] "s"(trash)
                : "vcc", "scc", "memory", "v48", "v49", "v50", "v51", "v52", "v53", "v54", "v55", "v56", "v57", "v58", "v59", "v60", "v61", "v120", "v121", "v126", "s72", "s73", "s74", "s75", "s76", "s77", "s78");
                }
            } else {
                gen_pass(it0, 0, cnt, true);
                gen_pass(it1, 1, cnt, true);
            }
            lds_signal(lds, P2_SYNC + par * 4, lane);
            lo += CAP;
            it_norm(p, lo);
        }
    } else {
        for (int i = t; i < (P2_CUR - P2_W) / 16; i += NT * 64) *(u32x4*)(lds + P2_W + i * 16) = u32x4{0u, 0u, 0u, 0u};
        if (t == 0) *(u32x2*)(lds + P2_NODES) = u32x2{OOB, 0u};
        if (t < 8) *(u32*)(lds + P2_SYNC + t * 4) = 0u;
        const auto rm = __builtin_amdgcn_make_buffer_rsrc((void*)m, 0, E_ * 32, 0x00020000);
        LDS_BARRIER();
        int totv = 0;
        if (lane < NPH) {
#pragma unroll
            for (int j = 0; j < 4; j++) {
                const u32x4 q4 = *(const u32x4*)(lds + P2_WTOT + (lane * 16 + j * 4) * 4);
                totv += q4[0] + q4[1] + q4[2] + q4[3];
            }
        }
        auto total_of = [&](int p) { return __builtin_amdgcn_readlane(totv, p); };
        auto it_norm = [&](int& p, int& lo) { while (p < NPH && lo >= total_of(p)) { p++; lo = 0; } };
        u32 wd0, wd1, wu0, wu1, ws0, wr0, cew;
        {
            const int i = lane & 15, q = lane >> 4;
            wd0 = w_down[(4 * q) * 16 + i] | ((u32)w_down[(4 * q + 1) * 16 + i] << 16);
            wd1 = w_down[(4 * q + 2) * 16 + i] | ((u32)w_down[(4 * q + 3) * 16 + i] << 16);
            wu0 = w_up[(4 * q) * 16 + i] | ((u32)w_up[(4 * q + 1) * 16 + i] << 16);
            wu1 = w_up[(4 * q + 2) * 16 + i] | ((u32)w_up[(4 * q + 3) * 16 + i] << 16);
            ws0 = w_sbf[(2 * q) * 16 + i] | ((u32)w_sbf[(2 * q + 1) * 16 + i] << 16);
            wr0 = w_rbf[(2 * q) * 16 + i] | ((u32)w_rbf[(2 * q + 1) * 16 + i] << 16);
            cew = centers[2 * q] | ((u32)centers[2 * q + 1] << 16);
            asm volatile("" : "+v"(wd0), "+v"(wd1), "+v"(wu0), "+v"(wu1), "+v"(ws0), "+v"(wr0), "+v"(cew));
        }
        const int i16 = lane & 15, q = lane >> 4;
        const u32 kw = 8 * q + (lane & 7);
        const u32 kl = 32 * ((lane >> 3) & 1) + kw;
        const u32 n2 = i16 * 2;
        const u32 wbase = (u32)(uintptr_t)(lds + P2_W) + wave * 16 * WRS;
        const u32 ww = wbase + kw * 2;
        const u32 wr = wbase + i16 * WRS + q * 16;
        f32x4 acc0 = {0.f, 0.f, 0.f, 0.f}, acc1 = acc0, acc2 = acc0, acc3 = acc0, acc4 = acc0, acc5 = acc0, acc6 = acc0, acc7 = acc0, acc8 = acc0, acc9 = acc0;
        int p = 0, lo = 0;
        it_norm(p, lo);
        for (int i = 0; p < NPH; i++) {
            const int par = i & 1;
            lds_wait_ge(lds, P2_SYNC + par * 4, NB * ((i >> 1) + 1));
            const u32 segv = *(const u32*)(lds + P2_SEG + ((i % 3) * 96 + wave * TPC + min(lane, TPC)) * 4);
            const u32 nxt = __builtin_amdgcn_update_dpp(0u, segv, 0x101, 0xf, 0xf, true);
            const u32 stp = lane < TPC ? (nxt - segv + 63) >> 6 : 0u;
            u32 sinc = stp;
            sinc += __builtin_amdgcn_update_dpp(0u, sinc, 0x111, 0xf, 0xf, true);
            sinc += __builtin_amdgcn_update_dpp(0u, sinc, 0x112, 0xf, 0xf, true);
            sinc += __builtin_amdgcn_update_dpp(0u, sinc, 0x114, 0xf, 0xf, true);
            sinc += __builtin_amdgcn_update_dpp(0u, sinc, 0x118, 0xf, 0xf, true);
            const u32 sfirst = sinc - stp;
            const int ns = __builtin_amdgcn_readlane(sinc, TPC - 1);
            const u32 tbase = segv - 64 * sfirst;
            for (int s0 = 0; s0 < ns; s0 += 60) {
                const int s1 = min(ns, s0 + 60);
                const u32 sl = s0 + lane;
                u32 k = 0;
#pragma unroll
                for (int j = 1; j < TPC; j++) k += sl >= (u32)__builtin_amdgcn_readlane(sfirst, j);
                const u32 tb = __shfl(tbase, k, 64), te = __shfl(nxt, k, 64);
                const u32 desc = (int)sl < s1 ? (tb + 64 * sl) | (te << 16) : 0u;
                const u32 cntv = max(min((int)sinc, s1) - max((int)sfirst, s0), 0);
                asm volatile(
                "s_waitcnt lgkmcnt(0)\n\t"
                "v_mov_b32 v42, 0\n\t"
                "s_mov_b32 s76, 0x00ff00ff\n\t"
                "s_mov_b32 s77, 0x00ff00ff\n\t"
                "s_mov_b32 s78, 0xff00ff00\n\t"
                "s_mov_b32 s79, 0xff00ff00\n\t"
                "s_mov_b32 s75, 0\n\t"
                "v_readlane_b32 s72, %[desc], s75\n\t"
                "s_add_u32 s75, s75, 1\n\t"
                "s_lshr_b32 s73, s72, 16\n\t"
                "s_and_b32 s72, s72, 0xffff\n\t"
                "s_nop 0\n\t"
                "v_add_u32 v39, s72, %[kl]\n\t"
                "v_cmp_gt_u32 vcc, s73, v39\n\t"
                "v_lshlrev_b32 v39, 3, v39\n\t"
                "s_nop 0\n\t"
                "v_cndmask_b32 v39, 0, v39, vcc\n\t"
                "ds_read_b64 v[52:53], v39\n\t"
                "v_readlane_b32 s72, %[desc], s75\n\t"
                "s_add_u32 s75, s75, 1\n\t"
                "s_lshr_b32 s73, s72, 16\n\t"
                "s_and_b32 s72, s72, 0xffff\n\t"
                "s_nop 0\n\t"
                "v_add_u32 v39, s72, %[kl]\n\t"
                "v_cmp_gt_u32 vcc, s73, v39\n\t"
                "v_lshlrev_b32 v39, 3, v39\n\t"
                "s_nop 0\n\t"
                "v_cndmask_b32 v39, 0, v39, vcc\n\t"
                "ds_read_b64 v[54:55], v39\n\t"
                "v_readlane_b32 s72, %[desc], s75\n\t"
                "s_add_u32 s75, s75, 1\n\t"
                "s_lshr_b32 s73, s72, 16\n\t"
                "s_and_b32 s72, s72, 0xffff\n\t"
                "s_waitcnt lgkmcnt(1)\n\t"
                "v_add_u32_dpp v40, v52, %[n2] row_newbcast:0 row_mask:0xf bank_mask:0xf\n\t"
                "v_add_u32_dpp v41, v52, %[n2] row_newbcast:1 row_mask:0xf bank_mask:0xf\n\t"
                "buffer_load_short_d16 v56, v40, %[rm], 0 offen\n\t"
                "v_add_u32_dpp v40, v52, %[n2] row_newbcast:2 row_mask:0xf bank_mask:0xf\n\t"
                "buffer_load_short_d16_hi v64, v41, %[rm], 0 offen\n\t"
                "v_add_u32_dpp v41, v52, %[n2] row_newbcast:3 row_mask:0xf bank_mask:0xf\n\t"
                "buffer_load_short_d16 v57, v40, %[rm], 0 offen\n\t"
                "v_add_u32_dpp v40, v52, %[n2] row_newbcast:4 row_mask:0xf bank_mask:0xf\n\t"
                "buffer_load_short_d16_hi v65, v41, %[rm], 0 offen\n\t"
                "v_add_u32_dpp v41, v52, %[n2] row_newbcast:5 row_mask:0xf bank_mask:0xf\n\t"
                "buffer_load_short_d16 v58, v40, %[rm], 0 offen\n\t"
                "v_add_u32_dpp v40, v52, %[n2] row_newbcast:6 row_mask:0xf bank_mask:0xf\n\t"
                "buffer_load_short_d16_hi v66, v41, %[rm], 0 offen\n\t"
                "v_add_u32_dpp v41, v52, %[n2] row_newbcast:7 row_mask:0xf bank_mask:0xf\n\t"
                "buffer_load_short_d16 v59, v40, %[rm], 0 offen\n\t"
                "v_add_u32_dpp v40, v52, %[n2] row_newbcast:8 row_mask:0xf bank_mask:0xf\n\t"
                "buffer_load_short_d16_hi v67, v41, %[rm], 0 offen\n\t"
                "v_add_u32_dpp v41, v52, %[n2] row_newbcast:9 row_mask:0xf bank_mask:0xf\n\t"
                "buffer_load_short_d16 v60, v40, %[rm], 0 offen\n\t"
                "v_add_u32_dpp v40, v52, %[n2] row_newbcast:10 row_mask:0xf bank_mask:0xf\n\t"
                "buffer_load_short_d16_hi v68, v41, %[rm], 0 offen\n\t"
                "v_add_u32_dpp v41, v52, %[n2] row_newbcast:11 row_mask:0xf bank_mask:0xf\n\t"
                "buffer_load_short_d16 v61, v40, %[rm], 0 offen\n\t"
                "v_add_u32_dpp v40, v52, %[n2] row_newbcast:12 row_mask:0xf bank_mask:0xf\n\t"
                "buffer_load_short_d16_hi v69, v41, %[rm], 0 offen\n\t"
                "v_add_u32_dpp v41, v52, %[n2] row_newbcast:13 row_mask:0xf bank_mask:0xf\n\t"
                "buffer_load_short_d16 v62, v40, %[rm], 0 offen\n\t"
                "v_add_u32_dpp v40, v52, %[n2] row_newbcast:14 row_mask:0xf bank_mask:0xf\n\t"
                "buffer_load_short_d16_hi v70, v41, %[rm], 0 offen\n\t"
                "v_add_u32_dpp v41, v52, %[n2] row_newbcast:15 row_mask:0xf bank_mask:0xf\n\t"
                "buffer_load_short_d16 v63, v40, %[rm], 0 offen\n\t"
                "buffer_load_short_d16_hi v71, v41, %[rm], 0 offen\n\t"
                "E0_0_%=:\n\t"
                "s_setprio 1\n\t"
                "v_readlane_b32 s74, %[cnt], 0\n\t"
                "s_nop 0\n\t"
                "s_cmp_eq_u32 s74, 0\n\t"
                "s_cbranch_scc1 E1_0_%=\n\t"
                "B0_0_%=:\n\t"
                "s_waitcnt lgkmcnt(0)\n\t"
                "v_add_u32_dpp v40, v54, %[n2] row_newbcast:0 row_mask:0xf bank_mask:0xf\n\t"
                "v_add_u32_dpp v41, v54, %[n2] row_newbcast:1 row_mask:0xf bank_mask:0xf\n\t"
                "buffer_load_short_d16 v72, v40, %[rm], 0 offen\n\t"
                "v_and_b32 v43, 0x3c, v53\n\t"
                "v_mad_u32_u24 v43, v43, 20, %[ww]\n\t"
                "v_add_u32_dpp v40, v54, %[n2] row_newbcast:2 row_mask:0xf bank_mask:0xf\n\t"
                "buffer_load_short_d16_hi v80, v41, %[rm], 0 offen\n\t"
                "v_add_u32_dpp v41, v54, %[n2] row_newbcast:3 row_mask:0xf bank_mask:0xf\n\t"
                "buffer_load_short_d16 v73, v40, %[rm], 0 offen\n\t"
                "s_mov_b64 exec, s[76:77]\n\t"
                "ds_write_b16_d16_hi v43, v53\n\t"
                "s_mov_b64 exec, -1\n\t"
                "v_add_u32_dpp v40, v54, %[n2] row_newbcast:4 row_mask:0xf bank_mask:0xf\n\t"
                "buffer_load_short_d16_hi v81, v41, %[rm], 0 offen\n\t"
                "ds_read_b128 v[44:47], %[wr]\n\t"
                "v_add_u32_dpp v41, v54, %[n2] row_newbcast:5 row_mask:0xf bank_mask:0xf\n\t"
                "buffer_load_short_d16 v74, v40, %[rm], 0 offen\n\t"
                "v_add_u32_dpp v40, v54, %[n2] row_newbcast:6 row_mask:0xf bank_mask:0xf\n\t"
                "buffer_load_short_d16_hi v82, v41, %[rm], 0 offen\n\t"
                "s_mov_b64 exec, s[76:77]\n\t"
                "ds_write_b16 v43, v42\n\t"
                "s_mov_b64 exec, s[78:79]\n\t"
                "ds_write_b16_d16_hi v43, v53\n\t"
                "s_mov_b64 exec, -1\n\t"
                "v_add_u32_dpp v41, v54, %[n2] row_newbcast:7 row_mask:0xf bank_mask:0xf\n\t"
                "buffer_load_short_d16 v75, v40, %[rm], 0 offen\n\t"
                "ds_read_b128 v[48:51], %[wr]\n\t"
                "v_add_u32_dpp v40, v54, %[n2] row_newbcast:8 row_mask:0xf bank_mask:0xf\n\t"
                "buffer_load_short_d16_hi v83, v41, %[rm], 0 offen\n\t"
                "v_add_u32_dpp v41, v54, %[n2] row_newbcast:9 row_mask:0xf bank_mask:0xf\n\t"
                "buffer_load_short_d16 v76, v40, %[rm], 0 offen\n\t"
                "s_mov_b64 exec, s[78:79]\n\t"
                "ds_write_b16 v43, v42\n\t"
                "s_mov_b64 exec, -1\n\t"
                "v_add_u32_dpp v40, v54, %[n2] row_newbcast:10 row_mask:0xf bank_mask:0xf\n\t"
                "buffer_load_short_d16_hi v84, v41, %[rm], 0 offen\n\t"
                "v_add_u32 v39, s72, %[kl]\n\t"
                "v_cmp_gt_u32 vcc, s73, v39\n\t"
                "v_add_u32_dpp v41, v54, %[n2] row_newbcast:11 row_mask:0xf bank_mask:0xf\n\t"
                "buffer_load_short_d16 v77, v40, %[rm], 0 offen\n\t"
                "v_add_u32_dpp v40, v54, %[n2] row_newbcast:12 row_mask:0xf bank_mask:0xf\n\t"
                "buffer_load_short_d16_hi v85, v41, %[rm], 0 offen\n\t"
                "v_lshlrev_b32 v39, 3, v39\n\t"
                "s_nop 0\n\t"
                "v_cndmask_b32 v39, 0, v39, vcc\n\t"
                "v_add_u32_dpp v41, v54, %[n2] row_newbcast:13 row_mask:0xf bank_mask:0xf\n\t"
                "buffer_load_short_d16 v78, v40, %[rm], 0 offen\n\t"
                "ds_read_b64 v[52:53], v39\n\t"
                "v_add_u32_dpp v40, v54, %[n2] row_newbcast:14 row_mask:0xf bank_mask:0xf\n\t"
                "buffer_load_short_d16_hi v86, v41, %[rm], 0 offen\n\t"
                "v_readlane_b32 s72, %[desc], s75\n\t"
                "s_add_u32 s75, s75, 1\n\t"
                "s_lshr_b32 s73, s72, 16\n\t"
                "s_and_b32 s72, s72, 0xffff\n\t"
                "v_add_u32_dpp v41, v54, %[n2] row_newbcast:15 row_mask:0xf bank_mask:0xf\n\t"
                "buffer_load_short_d16 v79, v40, %[rm], 0 offen\n\t"
                "buffer_load_short_d16_hi v87, v41, %[rm], 0 offen\n\t"
                "s_waitcnt vmcnt(16)\n\t"
                "v_or_b32 v56, v56, v64\n\t"
                "v_or_b32 v57, v57, v65\n\t"
                "v_or_b32 v58, v58, v66\n\t"
                "v_or_b32 v59, v59, v67\n\t"
                "v_or_b32 v60, v60, v68\n\t"
                "v_or_b32 v61, v61, v69\n\t"
                "v_or_b32 v62, v62, v70\n\t"
                "v_or_b32 v63, v63, v71\n\t"
                "s_waitcnt lgkmcnt(2)\n\t"
                "v_mfma_f32_16x16x32_bf16 v[88:91], v[56:59], v[44:47], v[88:91]\n\t"
                "v_mfma_f32_16x16x32_bf16 v[88:91], v[60:63], v[48:51], v[88:91]\n\t"
                "s_sub_u32 s74, s74, 1\n\t"
                "s_cmp_eq_u32 s74, 0\n\t"
                "s_cbranch_scc1 E1_1_%=\n\t"
                "B0_1_%=:\n\t"
                "s_waitcnt lgkmcnt(0)\n\t"
                "v_add_u32_dpp v40, v52, %[n2] row_newbcast:0 row_mask:0xf bank_mask:0xf\n\t"
                "v_add_u32_dpp v41, v52, %[n2] row_newbcast:1 row_mask:0xf bank_mask:0xf\n\t"
                "buffer_load_short_d16 v56, v40, %[rm], 0 offen\n\t"
                "v_and_b32 v43, 0x3c, v55\n\t"
                "v_mad_u32_u24 v43, v43, 20, %[ww]\n\t"
                "v_add_u32_dpp v40, v52, %[n2] row_newbcast:2 row_mask:0xf bank_mask:0xf\n\t"
                "buffer_load_short_d16_hi v64, v41, %[rm], 0 offen\n\t"
                "v_add_u32_dpp v41, v52, %[n2] row_newbcast:3 row_mask:0xf bank_mask:0xf\n\t"
                "buffer_load_short_d16 v57, v40, %[rm], 0 offen\n\t"
                "s_mov_b64 exec, s[76:77]\n\t"
                "ds_write_b16_d16_hi v43, v55\n\t"
                "s_mov_b64 exec, -1\n\t"
                "v_add_u32_dpp v40, v52, %[n2] row_newbcast:4 row_mask:0xf bank_mask:0xf\n\t"
                "buffer_load_short_d16_hi v65, v41, %[rm], 0 offen\n\t"
                "ds_read_b128 v[44:47], %[wr]\n\t"
                "v_add_u32_dpp v41, v52, %[n2] row_newbcast:5 row_mask:0xf bank_mask:0xf\n\t"
                "buffer_load_short_d16 v58, v40, %[rm], 0 offen\n\t"
                "v_add_u32_dpp v40, v52, %[n2] row_newbcast:6 row_mask:0xf bank_mask:0xf\n\t"
                "buffer_load_short_d16_hi v66, v41, %[rm], 0 offen\n\t"
                "s_mov_b64 exec, s[76:77]\n\t"
                "ds_write_b16 v43, v42\n\t"
                "s_mov_b64 exec, s[78:79]\n\t"
                "ds_write_b16_d16_hi v43, v55\n\t"
                "s_mov_b64 exec, -1\n\t"
                "v_add_u32_dpp v41, v52, %[n2] row_newbcast:7 row_mask:0xf bank_mask:0xf\n\t"
                "buffer_load_short_d16 v59, v40, %[rm], 0 offen\n\t"
                "ds_read_b128 v[48:51], %[wr]\n\t"
                "v_add_u32_dpp v40, v52, %[n2] row_newbcast:8 row_mask:0xf bank_mask:0xf\n\t"
                "buffer_load_short_d16_hi v67, v41, %[rm], 0 offen\n\t"
                "v_add_u32_dpp v41, v52, %[n2] row_newbcast:9 row_mask:0xf bank_mask:0xf\n\t"
                "buffer_load_short_d16 v60, v40, %[rm], 0 offen\n\t"
                "s_mov_b64 exec, s[78:79]\n\t"
                "ds_write_b16 v43, v42\n\t"
                "s_mov_b64 exec, -1\n\t"
                "v_add_u32_dpp v40, v52, %[n2] row_newbcast:10 row_mask:0xf bank_mask:0xf\n\t"
                "buffer_load_short_d16_hi v68, v41, %[rm], 0 offen\n\t"
                "v_add_u32 v39, s72, %[kl]\n\t"
                "v_cmp_gt_u32 vcc, s73, v39\n\t"
                "v_add_u32_dpp v41, v52, %[n2] row_newbcast:11 row_mask:0xf bank_mask:0xf\n\t"
                "buffer_load_short_d16 v61, v40, %[rm], 0 offen\n\t"
                "v_add_u32_dpp v40, v52, %[n2] row_newbcast:12 row_mask:0xf bank_mask:0xf\n\t"
                "buffer_load_short_d16_hi v69, v41, %[rm], 0 offen\n\t"
                "v_lshlrev_b32 v39, 3, v39\n\t"
                "s_nop 0\n\t"
                "v_cndmask_b32 v39, 0, v39, vcc\n\t"
                "v_add_u32_dpp v41, v52, %[n2] row_newbcast:13 row_mask:0xf bank_mask:0xf\n\t"
                "buffer_load_short_d16 v62, v40, %[rm], 0 offen\n\t"
                "ds_read_b64 v[54:55], v39\n\t"
                "v_add_u32_dpp v40, v52, %[n2] row_newbcast:14 row_mask:0xf bank_mask:0xf\n\t"
                "buffer_load_short_d16_hi v70, v41, %[rm], 0 offen\n\t"
                "v_readlane_b32 s72, %[desc], s75\n\t"
                "s_add_u32 s75, s75, 1\n\t"
                "s_lshr_b32 s73, s72, 16\n\t"
                "s_and_b32 s72, s72, 0xffff\n\t"
                "v_add_u32_dpp v41, v52, %[n2] row_newbcast:15 row_mask:0xf bank_mask:0xf\n\t"
                "buffer_load_short_d16 v63, v40, %[rm], 0 offen\n\t"
                "buffer_load_short_d16_hi v71, v41, %[rm], 0 offen\n\t"
                "s_waitcnt vmcnt(16)\n\t"
                "v_or_b32 v72, v72, v80\n\t"
                "v_or_b32 v73, v73, v81\n\t"
                "v_or_b32 v74, v74, v82\n\t"
                "v_or_b32 v75, v75, v83\n\t"
                "v_or_b32 v76, v76, v84\n\t"
                "v_or_b32 v77, v77, v85\n\t"
                "v_or_b32 v78, v78, v86\n\t"
                "v_or_b32 v79, v79, v87\n\t"
                "s_waitcnt lgkmcnt(2)\n\t"
                "v_mfma_f32_16x16x32_bf16 v[88:91], v[72:75], v[44:47], v[88:91]\n\t"
                "v_mfma_f32_16x16x32_bf16 v[88:91], v[76:79], v[48:51], v[88:91]\n\t"
                "s_sub_u32 s74, s74, 1\n\t"
                "s_cmp_eq_u32 s74, 0\n\t"
                "s_cbranch_scc0 B0_0_%=\n\t"
                "s_branch E1_0_%=\n\t"
                "E0_1_%=:\n\t"
                "s_setprio 1\n\t"
                "v_readlane_b32 s74, %[cnt], 0\n\t"
                "s_nop 0\n\t"
                "s_cmp_eq_u32 s74, 0\n\t"
                "s_cbranch_scc0 B0_1_%=\n\t"
                "s_branch E1_1_%=\n\t"
                "E1_0_%=:\n\t"
                "s_setprio 1\n\t"
                "v_readlane_b32 s74, %[cnt], 1\n\t"
                "s_nop 0\n\t"
                "s_cmp_eq_u32 s74, 0\n\t"
                "s_cbranch_scc1 E2_0_%=\n\t"
                "B1_0_%=:\n\t"
                "s_waitcnt lgkmcnt(0)\n\t"
                "v_add_u32_dpp v40, v54, %[n2] row_newbcast:0 row_mask:0xf bank_mask:0xf\n\t"
                "v_add_u32_dpp v41, v54, %[n2] row_newbcast:1 row_mask:0xf bank_mask:0xf\n\t"
                "buffer_load_short_d16 v72, v40, %[rm], 0 offen\n\t"
                "v_and_b32 v43, 0x3c, v53\n\t"
                "v_mad_u32_u24 v43, v43, 20, %[ww]\n\t"
                "v_add_u32_dpp v40, v54, %[n2] row_newbcast:2 row_mask:0xf bank_mask:0xf\n\t"
                "buffer_load_short_d16_hi v80, v41, %[rm], 0 offen\n\t"
                "v_add_u32_dpp v41, v54, %[n2] row_newbcast:3 row_mask:0xf bank_mask:0xf\n\t"
                "buffer_load_short_d16 v73, v40, %[rm], 0 offen\n\t"
                "s_mov_b64 exec, s[76:77]\n\t"
                "ds_write_b16_d16_hi v43, v53\n\t"
                "s_mov_b64 exec, -1\n\t"
                "v_add_u32_dpp v40, v54, %[n2] row_newbcast:4 row_mask:0xf bank_mask:0xf\n\t"
                "buffer_load_short_d16_hi v81, v41, %[rm], 0 offen\n\t"
                "ds_read_b128 v[44:47], %[wr]\n\t"
                "v_add_u32_dpp v41, v54, %[n2] row_newbcast:5 row_mask:0xf bank_mask:0xf\n\t"
                "buffer_load_short_d16 v74, v40, %[rm], 0 offen\n\t"
                "v_add_u32_dpp v40, v54, %[n2] row_newbcast:6 row_mask:0xf bank_mask:0xf\n\t"
                "buffer_load_short_d16_hi v82, v41, %[rm], 0 offen\n\t"
                "s_mov_b64 exec, s[76:77]\n\t"
                "ds_write_b16 v43, v42\n\t"
                "s_mov_b64 exec, s[78:79]\n\t"
                "ds_write_b16_d16_hi v43, v53\n\t"
                "s_mov_b64 exec, -1\n\t"
                "v_add_u32_dpp v41, v54, %[n2] row_newbcast:7 row_mask:0xf bank_mask:0xf\n\t"
                "buffer_load_short_d16 v75, v40, %[rm], 0 offen\n\t"
                "ds_read_b128 v[48:51], %[wr]\n\t"
                "v_add_u32_dpp v40, v54, %[n2] row_newbcast:8 row_mask:0xf bank_mask:0xf\n\t"
                "buffer_load_short_d16_hi v83, v41, %[rm], 0 offen\n\t"
                "v_add_u32_dpp v41, v54, %[n2] row_newbcast:9 row_mask:0xf bank_mask:0xf\n\t"
                "buffer_load_short_d16 v76, v40, %[rm], 0 offen\n\t"
                "s_mov_b64 exec, s[78:79]\n\t"
                "ds_write_b16 v43, v42\n\t"
                "s_mov_b64 exec, -1\n\t"
                "v_add_u32_dpp v40, v54, %[n2] row_newbcast:10 row_mask:0xf bank_mask:0xf\n\t"
                "buffer_load_short_d16_hi v84, v41, %[rm], 0 offen\n\t"
                "v_add_u32 v39, s72, %[kl]\n\t"
                "v_cmp_gt_u32 vcc, s73, v39\n\t"
                "v_add_u32_dpp v41, v54, %[n2] row_newbcast:11 row_mask:0xf bank_mask:0xf\n\t"
                "buffer_load_short_d16 v77, v40, %[rm], 0 offen\n\t"
                "v_add_u32_dpp v40, v54, %[n2] row_newbcast:12 row_mask:0xf bank_mask:0xf\n\t"
                "buffer_load_short_d16_hi v85, v41, %[rm], 0 offen\n\t"
                "v_lshlrev_b32 v39, 3, v39\n\t"
                "s_nop 0\n\t"
                "v_cndmask_b32 v39, 0, v39, vcc\n\t"
                "v_add_u32_dpp v41, v54, %[n2] row_newbcast:13 row_mask:0xf bank_mask:0xf\n\t"
                "buffer_load_short_d16 v78, v40, %[rm], 0 offen\n\t"
                "ds_read_b64 v[52:53], v39\n\t"
                "v_add_u32_dpp v40, v54, %[n2] row_newbcast:14 row_mask:0xf bank_mask:0xf\n\t"
                "buffer_load_short_d16_hi v86, v41, %[rm], 0 offen\n\t"
                "v_readlane_b32 s72, %[desc], s75\n\t"
                "s_add_u32 s75, s75, 1\n\t"
                "s_lshr_b32 s73, s72, 16\n\t"
                "s_and_b32 s72, s72, 0xffff\n\t"
                "v_add_u32_dpp v41, v54, %[n2] row_newbcast:15 row_mask:0xf bank_mask:0xf\n\t"
                "buffer_load_short_d16 v79, v40, %[rm], 0 offen\n\t"
                "buffer_load_short_d16_hi v87, v41, %[rm], 0 offen\n\t"
                "s_waitcnt vmcnt(16)\n\t"
                "v_or_b32 v56, v56, v64\n\t"
                "v_or_b32 v57, v57, v65\n\t"
                "v_or_b32 v58, v58, v66\n\t"
                "v_or_b32 v59, v59, v67\n\t"
                "v_or_b32 v60, v60, v68\n\t"
                "v_or_b32 v61, v61, v69\n\t"
                "v_or_b32 v62, v62, v70\n\t"
                "v_or_b32 v63, v63, v71\n\t"
                "s_waitcnt lgkmcnt(2)\n\t"
                "v_mfma_f32_16x16x32_bf16 v[92:95], v[56:59], v[44:47], v[92:95]\n\t"
                "v_mfma_f32_16x16x32_bf16 v[92:95], v[60:63], v[48:51], v[92:95]\n\t"
                "s_sub_u32 s74, s74, 1\n\t"
                "s_cmp_eq_u32 s74, 0\n\t"
                "s_cbranch_scc1 E2_1_%=\n\t"
                "B1_1_%=:\n\t"
                "s_waitcnt lgkmcnt(0)\n\t"
                "v_add_u32_dpp v40, v52, %[n2] row_newbcast:0 row_mask:0xf bank_mask:0xf\n\t"
                "v_add_u32_dpp v41, v52, %[n2] row_newbcast:1 row_mask:0xf bank_mask:0xf\n\t"
                "buffer_load_short_d16 v56, v40, %[rm], 0 offen\n\t"
                "v_and_b32 v43, 0x3c, v55\n\t"
                "v_mad_u32_u24 v43, v43, 20, %[ww]\n\t"
                "v_add_u32_dpp v40, v52, %[n2] row_newbcast:2 row_mask:0xf bank_mask:0xf\n\t"
                "buffer_load_short_d16_hi v64, v41, %[rm], 0 offen\n\t"
                "v_add_u32_dpp v41, v52, %[n2] row_newbcast:3 row_mask:0xf bank_mask:0xf\n\t"
                "buffer_load_short_d16 v57, v40, %[rm], 0 offen\n\t"
                "s_mov_b64 exec, s[76:77]\n\t"
                "ds_write_b16_d16_hi v43, v55\n\t"
                "s_mov_b64 exec, -1\n\t"
                "v_add_u32_dpp v40, v52, %[n2] row_newbcast:4 row_mask:0xf bank_mask:0xf\n\t"
                "buffer_load_short_d16_hi v65, v41, %[rm], 0 offen\n\t"
                "ds_read_b128 v[44:47], %[wr]\n\t"
                "v_add_u32_dpp v41, v52, %[n2] row_newbcast:5 row_mask:0xf bank_mask:0xf\n\t"
                "buffer_load_short_d16 v58, v40, %[rm], 0 offen\n\t"
                "v_add_u32_dpp v40, v52, %[n2] row_newbcast:6 row_mask:0xf bank_mask:0xf\n\t"
                "buffer_load_short_d16_hi v66, v41, %[rm], 0 offen\n\t"
                "s_mov_b64 exec, s[76:77]\n\t"
                "ds_write_b16 v43, v42\n\t"
                "s_mov_b64 exec, s[78:79]\n\t"
                "ds_write_b16_d16_hi v43, v55\n\t"
                "s_mov_b64 exec, -1\n\t"
                "v_add_u32_dpp v41, v52, %[n2] row_newbcast:7 row_mask:0xf bank_mask:0xf\n\t"
                "buffer_load_short_d16 v59, v40, %[rm], 0 offen\n\t"
                "ds_read_b128 v[48:51], %[wr]\n\t"
                "v_add_u32_dpp v40, v52, %[n2] row_newbcast:8 row_mask:0xf bank_mask:0xf\n\t"
                "buffer_load_short_d16_hi v67, v41, %[rm], 0 offen\n\t"
                "v_add_u32_dpp v41, v52, %[n2] row_newbcast:9 row_mask:0xf bank_mask:0xf\n\t"
                "buffer_load_short_d16 v60, v40, %[rm], 0 offen\n\t"
                "s_mov_b64 exec, s[78:79]\n\t"
                "ds_write_b16 v43, v42\n\t"
                "s_mov_b64 exec, -1\n\t"
                "v_add_u32_dpp v40, v52, %[n2] row_newbcast:10 row_mask:0xf bank_mask:0xf\n\t"
                "buffer_load_short_d16_hi v68, v41, %[rm], 0 offen\n\t"
                "v_add_u32 v39, s72, %[kl]\n\t"
                "v_cmp_gt_u32 vcc, s73, v39\n\t"
                "v_add_u32_dpp v41, v52, %[n2] row_newbcast:11 row_mask:0xf bank_mask:0xf\n\t"
                "buffer_load_short_d16 v61, v40, %[rm], 0 offen\n\t"
                "v_add_u32_dpp v40, v52, %[n2] row_newbcast:12 row_mask:0xf bank_mask:0xf\n\t"
                "buffer_load_short_d16_hi v69, v41, %[rm], 0 offen\n\t"
                "v_lshlrev_b32 v39, 3, v39\n\t"
                "s_nop 0\n\t"
                "v_cndmask_b32 v39, 0, v39, vcc\n\t"
                "v_add_u32_dpp v41, v52, %[n2] row_newbcast:13 row_mask:0xf bank_mask:0xf\n\t"
                "buffer_load_short_d16 v62, v40, %[rm], 0 offen\n\t"
                "ds_read_b64 v[54:55], v39\n\t"
                "v_add_u32_dpp v40, v52, %[n2] row_newbcast:14 row_mask:0xf bank_mask:0xf\n\t"
                "buffer_load_short_d16_hi v70, v41, %[rm], 0 offen\n\t"
                "v_readlane_b32 s72, %[desc], s75\n\t"
                "s_add_u32 s75, s75, 1\n\t"
                "s_lshr_b32 s73, s72, 16\n\t"
                "s_and_b32 s72, s72, 0xffff\n\t"
                "v_add_u32_dpp v41, v52, %[n2] row_newbcast:15 row_mask:0xf bank_mask:0xf\n\t"
                "buffer_load_short_d16 v63, v40, %[rm], 0 offen\n\t"
                "buffer_load_short_d16_hi v71, v41, %[rm], 0 offen\n\t"
                "s_waitcnt vmcnt(16)\n\t"
                "v_or_b32 v72, v72, v80\n\t"
                "v_or_b32 v73, v73, v81\n\t"
                "v_or_b32 v74, v74, v82\n\t"
                "v_or_b32 v75, v75, v83\n\t"
                "v_or_b32 v76, v76, v84\n\t"
                "v_or_b32 v77, v77, v85\n\t"
                "v_or_b32 v78, v78, v86\n\t"
                "v_or_b32 v79, v79, v87\n\t"
                "s_waitcnt lgkmcnt(2)\n\t"
                "v_mfma_f32_16x16x32_bf16 v[92:95], v[72:75], v[44:47], v[92:95]\n\t"
                "v_mfma_f32_16x16x32_bf16 v[92:95], v[76:79], v[48:51], v[92:95]\n\t"
                "s_sub_u32 s74, s74, 1\n\t"
                "s_cmp_eq_u32 s74, 0\n\t"
                "s_cbranch_scc0 B1_0_%=\n\t"
                "s_branch E2_0_%=\n\t"
                "E1_1_%=:\n\t"
                "s_setprio 1\n\t"
                "v_readlane_b32 s74, %[cnt], 1\n\t"
                "s_nop 0\n\t"
                "s_cmp_eq_u32 s74, 0\n\t"
                "s_cbranch_scc0 B1_1_%=\n\t"
                "s_branch E2_1_%=\n\t"
                "E2_0_%=:\n\t"
                "s_setprio 1\n\t"
                "v_readlane_b32 s74, %[cnt], 2\n\t"
                "s_nop 0\n\t"
                "s_cmp_eq_u32 s74, 0\n\t"
                "s_cbranch_scc1 E3_0_%=\n\t"
                "B2_0_%=:\n\t"
                "s_waitcnt lgkmcnt(0)\n\t"
                "v_add_u32_dpp v40, v54, %[n2] row_newbcast:0 row_mask:0xf bank_mask:0xf\n\t"
                "v_add_u32_dpp v41, v54, %[n2] row_newbcast:1 row_mask:0xf bank_mask:0xf\n\t"
                "buffer_load_short_d16 v72, v40, %[rm], 0 offen\n\t"
                "v_and_b32 v43, 0x3c, v53\n\t"
                "v_mad_u32_u24 v43, v43, 20, %[ww]\n\t"
                "v_add_u32_dpp v40, v54, %[n2] row_newbcast:2 row_mask:0xf bank_mask:0xf\n\t"
                "buffer_load_short_d16_hi v80, v41, %[rm], 0 offen\n\t"
                "v_add_u32_dpp v41, v54, %[n2] row_newbcast:3 row_mask:0xf bank_mask:0xf\n\t"
                "buffer_load_short_d16 v73, v40, %[rm], 0 offen\n\t"
                "s_mov_b64 exec, s[76:77]\n\t"
                "ds_write_b16_d16_hi v43, v53\n\t"
                "s_mov_b64 exec, -1\n\t"
                "v_add_u32_dpp v40, v54, %[n2] row_newbcast:4 row_mask:0xf bank_mask:0xf\n\t"
                "buffer_load_short_d16_hi v81, v41, %[rm], 0 offen\n\t"
                "ds_read_b128 v[44:47], %[wr]\n\t"
                "v_add_u32_dpp v41, v54, %[n2] row_newbcast:5 row_mask:0xf bank_mask:0xf\n\t"
                "buffer_load_short_d16 v74, v40, %[rm], 0 offen\n\t"
                "v_add_u32_dpp v40, v54, %[n2] row_newbcast:6 row_mask:0xf bank_mask:0xf\n\t"
                "buffer_load_short_d16_hi v82, v41, %[rm], 0 offen\n\t"
                "s_mov_b64 exec, s[76:77]\n\t"
                "ds_write_b16 v43, v42\n\t"
                "s_mov_b64 exec, s[78:79]\n\t"
                "ds_write_b16_d16_hi v43, v53\n\t"
                "s_mov_b64 exec, -1\n\t"
                "v_add_u32_dpp v41, v54, %[n2] row_newbcast:7 row_mask:0xf bank_mask:0xf\n\t"
                "buffer_load_short_d16 v75, v40, %[rm], 0 offen\n\t"
                "ds_read_b128 v[48:51], %[wr]\n\t"
                "v_add_u32_dpp v40, v54, %[n2] row_newbcast:8 row_mask:0xf bank_mask:0xf\n\t"
                "buffer_load_short_d16_hi v83, v41, %[rm], 0 offen\n\t"
                "v_add_u32_dpp v41, v54, %[n2] row_newbcast:9 row_mask:0xf bank_mask:0xf\n\t"
                "buffer_load_short_d16 v76, v40, %[rm], 0 offen\n\t"
                "s_mov_b64 exec, s[78:79]\n\t"
                "ds_write_b16 v43, v42\n\t"
                "s_mov_b64 exec, -1\n\t"
                "v_add_u32_dpp v40, v54, %[n2] row_newbcast:10 row_mask:0xf bank_mask:0xf\n\t"
                "buffer_load_short_d16_hi v84, v41, %[rm], 0 offen\n\t"
                "v_add_u32 v39, s72, %[kl]\n\t"
                "v_cmp_gt_u32 vcc, s73, v39\n\t"
                "v_add_u32_dpp v41, v54, %[n2] row_newbcast:11 row_mask:0xf bank_mask:0xf\n\t"
                "buffer_load_short_d16 v77, v40, %[rm], 0 offen\n\t"
                "v_add_u32_dpp v40, v54, %[n2] row_newbcast:12 row_mask:0xf bank_mask:0xf\n\t"
                "buffer_load_short_d16_hi v85, v41, %[rm], 0 offen\n\t"
                "v_lshlrev_b32 v39, 3, v39\n\t"
                "s_nop 0\n\t"
                "v_cndmask_b32 v39, 0, v39, vcc\n\t"
                "v_add_u32_dpp v41, v54, %[n2] row_newbcast:13 row_mask:0xf bank_mask:0xf\n\t"
                "buffer_load_short_d16 v78, v40, %[rm], 0 offen\n\t"
                "ds_read_b64 v[52:53], v39\n\t"
                "v_add_u32_dpp v40, v54, %[n2] row_newbcast:14 row_mask:0xf bank_mask:0xf\n\t"
                "buffer_load_short_d16_hi v86, v41, %[rm], 0 offen\n\t"
                "v_readlane_b32 s72, %[desc], s75\n\t"
                "s_add_u32 s75, s75, 1\n\t"
                "s_lshr_b32 s73, s72, 16\n\t"
                "s_and_b32 s72, s72, 0xffff\n\t"
                "v_add_u32_dpp v41, v54, %[n2] row_newbcast:15 row_mask:0xf bank_mask:0xf\n\t"
                "buffer_load_short_d16 v79, v40, %[rm], 0 offen\n\t"
                "buffer_load_short_d16_hi v87, v41, %[rm], 0 offen\n\t"
                "s_waitcnt vmcnt(16)\n\t"
                "v_or_b32 v56, v56, v64\n\t"
                "v_or_b32 v57, v57, v65\n\t"
                "v_or_b32 v58, v58, v66\n\t"
                "v_or_b32 v59, v59, v67\n\t"
                "v_or_b32 v60, v60, v68\n\t"
                "v_or_b32 v61, v61, v69\n\t"
                "v_or_b32 v62, v62, v70\n\t"
                "v_or_b32 v63, v63, v71\n\t"
                "s_waitcnt lgkmcnt(2)\n\t"
                "v_mfma_f32_16x16x32_bf16 v[96:99], v[56:59], v[44:47], v[96:99]\n\t"
                "v_mfma_f32_16x16x32_bf16 v[96:99], v[60:63], v[48:51], v[96:99]\n\t"
                "s_sub_u32 s74, s74, 1\n\t"
                "s_cmp_eq_u32 s74, 0\n\t"
                "s_cbranch_scc1 E3_1_%=\n\t"
                "B2_1_%=:\n\t"
                "s_waitcnt lgkmcnt(0)\n\t"
                "v_add_u32_dpp v40, v52, %[n2] row_newbcast:0 row_mask:0xf bank_mask:0xf\n\t"
                "v_add_u32_dpp v41, v52, %[n2] row_newbcast:1 row_mask:0xf bank_mask:0xf\n\t"
                "buffer_load_short_d16 v56, v40, %[rm], 0 offen\n\t"
                "v_and_b32 v43, 0x3c, v55\n\t"
                "v_mad_u32_u24 v43, v43, 20, %[ww]\n\t"
                "v_add_u32_dpp v40, v52, %[n2] row_newbcast:2 row_mask:0xf bank_mask:0xf\n\t"
                "buffer_load_short_d16_hi v64, v41, %[rm], 0 offen\n\t"
                "v_add_u32_dpp v41, v52, %[n2] row_newbcast:3 row_mask:0xf bank_mask:0xf\n\t"
                "buffer_load_short_d16 v57, v40, %[rm], 0 offen\n\t"
                "s_mov_b64 exec, s[76:77]\n\t"
                "ds_write_b16_d16_hi v43, v55\n\t"
                "s_mov_b64 exec, -1\n\t"
                "v_add_u32_dpp v40, v52, %[n2] row_newbcast:4 row_mask:0xf bank_mask:0xf\n\t"
                "buffer_load_short_d16_hi v65, v41, %[rm], 0 offen\n\t"
                "ds_read_b128 v[44:47], %[wr]\n\t"
                "v_add_u32_dpp v41, v52, %[n2] row_newbcast:5 row_mask:0xf bank_mask:0xf\n\t"
                "buffer_load_short_d16 v58, v40, %[rm], 0 offen\n\t"
                "v_add_u32_dpp v40, v52, %[n2] row_newbcast:6 row_mask:0xf bank_mask:0xf\n\t"
                "buffer_load_short_d16_hi v66, v41, %[rm], 0 offen\n\t"
                "s_mov_b64 exec, s[76:77]\n\t"
                "ds_write_b16 v43, v42\n\t"
                "s_mov_b64 exec, s[78:79]\n\t"
                "ds_write_b16_d16_hi v43, v55\n\t"
                "s_mov_b64 exec, -1\n\t"
                "v_add_u32_dpp v41, v52, %[n2] row_newbcast:7 row_mask:0xf bank_mask:0xf\n\t"
                "buffer_load_short_d16 v59, v40, %[rm], 0 offen\n\t"
                "ds_read_b128 v[48:51], %[wr]\n\t"
                "v_add_u32_dpp v40, v52, %[n2] row_newbcast:8 row_mask:0xf bank_mask:0xf\n\t"
                "buffer_load_short_d16_hi v67, v41, %[rm], 0 offen\n\t"
                "v_add_u32_dpp v41, v52, %[n2] row_newbcast:9 row_mask:0xf bank_mask:0xf\n\t"
                "buffer_load_short_d16 v60, v40, %[rm], 0 offen\n\t"
                "s_mov_b64 exec, s[78:79]\n\t"
                "ds_write_b16 v43, v42\n\t"
                "s_mov_b64 exec, -1\n\t"
                "v_add_u32_dpp v40, v52, %[n2] row_newbcast:10 row_mask:0xf bank_mask:0xf\n\t"
                "buffer_load_short_d16_hi v68, v41, %[rm], 0 offen\n\t"
                "v_add_u32 v39, s72, %[kl]\n\t"
                "v_cmp_gt_u32 vcc, s73, v39\n\t"
                "v_add_u32_dpp v41, v52, %[n2] row_newbcast:11 row_mask:0xf bank_mask:0xf\n\t"
                "buffer_load_short_d16 v61, v40, %[rm], 0 offen\n\t"
                "v_add_u32_dpp v40, v52, %[n2] row_newbcast:12 row_mask:0xf bank_mask:0xf\n\t"
                "buffer_load_short_d16_hi v69, v41, %[rm], 0 offen\n\t"
                "v_lshlrev_b32 v39, 3, v39\n\t"
                "s_nop 0\n\t"
                "v_cndmask_b32 v39, 0, v39, vcc\n\t"
                "v_add_u32_dpp v41, v52, %[n2] row_newbcast:13 row_mask:0xf bank_mask:0xf\n\t"
                "buffer_load_short_d16 v62, v40, %[rm], 0 offen\n\t"
                "ds_read_b64 v[54:55], v39\n\t"
                "v_add_u32_dpp v40, v52, %[n2] row_newbcast:14 row_mask:0xf bank_mask:0xf\n\t"
                "buffer_load_short_d16_hi v70, v41, %[rm], 0 offen\n\t"
                "v_readlane_b32 s72, %[desc], s75\n\t"
                "s_add_u32 s75, s75, 1\n\t"
                "s_lshr_b32 s73, s72, 16\n\t"
                "s_and_b32 s72, s72, 0xffff\n\t"
                "v_add_u32_dpp v41, v52, %[n2] row_newbcast:15 row_mask:0xf bank_mask:0xf\n\t"
                "buffer_load_short_d16 v63, v40, %[rm], 0 offen\n\t"
                "buffer_load_short_d16_hi v71, v41, %[rm], 0 offen\n\t"
                "s_waitcnt vmcnt(16)\n\t"
                "v_or_b32 v72, v72, v80\n\t"
                "v_or_b32 v73, v73, v81\n\t"
                "v_or_b32 v74, v74, v82\n\t"
                "v_or_b32 v75, v75, v83\n\t"
                "v_or_b32 v76, v76, v84\n\t"
                "v_or_b32 v77, v77, v85\n\t"
                "v_or_b32 v78, v78, v86\n\t"
                "v_or_b32 v79, v79, v87\n\t"
                "s_waitcnt lgkmcnt(2)\n\t"
                "v_mfma_f32_16x16x32_bf16 v[96:99], v[72:75], v[44:47], v[96:99]\n\t"
                "v_mfma_f32_16x16x32_bf16 v[96:99], v[76:79], v[48:51], v[96:99]\n\t"
                "s_sub_u32 s74, s74, 1\n\t"
                "s_cmp_eq_u32 s74, 0\n\t"
                "s_cbranch_scc0 B2_0_%=\n\t"
                "s_branch E3_0_%=\n\t"
                "E2_1_%=:\n\t"
                "s_setprio 1\n\t"
                "v_readlane_b32 s74, %[cnt], 2\n\t"
                "s_nop 0\n\t"
                "s_cmp_eq_u32 s74, 0\n\t"
                "s_cbranch_scc0 B2_1_%=\n\t"
                "s_branch E3_1_%=\n\t"
                "E3_0_%=:\n\t"
                "s_setprio 1\n\t"
                "v_readlane_b32 s74, %[cnt], 3\n\t"
                "s_nop 0\n\t"
                "s_cmp_eq_u32 s74, 0\n\t"
                "s_cbranch_scc1 E4_0_%=\n\t"
                "B3_0_%=:\n\t"
                "s_waitcnt lgkmcnt(0)\n\t"
                "v_add_u32_dpp v40, v54, %[n2] row_newbcast:0 row_mask:0xf bank_mask:0xf\n\t"
                "v_add_u32_dpp v41, v54, %[n2] row_newbcast:1 row_mask:0xf bank_mask:0xf\n\t"
                "buffer_load_short_d16 v72, v40, %[rm], 0 offen\n\t"
                "v_and_b32 v43, 0x3c, v53\n\t"
                "v_mad_u32_u24 v43, v43, 20, %[ww]\n\t"
                "v_add_u32_dpp v40, v54, %[n2] row_newbcast:2 row_mask:0xf bank_mask:0xf\n\t"
                "buffer_load_short_d16_hi v80, v41, %[rm], 0 offen\n\t"
                "v_add_u32_dpp v41, v54, %[n2] row_newbcast:3 row_mask:0xf bank_mask:0xf\n\t"
                "buffer_load_short_d16 v73, v40, %[rm], 0 offen\n\t"
                "s_mov_b64 exec, s[76:77]\n\t"
                "ds_write_b16_d16_hi v43, v53\n\t"
                "s_mov_b64 exec, -1\n\t"
                "v_add_u32_dpp v40, v54, %[n2] row_newbcast:4 row_mask:0xf bank_mask:0xf\n\t"
                "buffer_load_short_d16_hi v81, v41, %[rm], 0 offen\n\t"
                "ds_read_b128 v[44:47], %[wr]\n\t"
                "v_add_u32_dpp v41, v54, %[n2] row_newbcast:5 row_mask:0xf bank_mask:0xf\n\t"
                "buffer_load_short_d16 v74, v40, %[rm], 0 offen\n\t"
                "v_add_u32_dpp v40, v54, %[n2] row_newbcast:6 row_mask:0xf bank_mask:0xf\n\t"
                "buffer_load_short_d16_hi v82, v41, %[rm], 0 offen\n\t"
                "s_mov_b64 exec, s[76:77]\n\t"
                "ds_write_b16 v43, v42\n\t"
                "s_mov_b64 exec, s[78:79]\n\t"
                "ds_write_b16_d16_hi v43, v53\n\t"
                "s_mov_b64 exec, -1\n\t"
                "v_add_u32_dpp v41, v54, %[n2] row_newbcast:7 row_mask:0xf bank_mask:0xf\n\t"
                "buffer_load_short_d16 v75, v40, %[rm], 0 offen\n\t"
                "ds_read_b128 v[48:51], %[wr]\n\t"
                "v_add_u32_dpp v40, v54, %[n2] row_newbcast:8 row_mask:0xf bank_mask:0xf\n\t"
                "buffer_load_short_d16_hi v83, v41, %[rm], 0 offen\n\t"
                "v_add_u32_dpp v41, v54, %[n2] row_newbcast:9 row_mask:0xf bank_mask:0xf\n\t"
                "buffer_load_short_d16 v76, v40, %[rm], 0 offen\n\t"
                "s_mov_b64 exec, s[78:79]\n\t"
                "ds_write_b16 v43, v42\n\t"
                "s_mov_b64 exec, -1\n\t"
                "v_add_u32_dpp v40, v54, %[n2] row_newbcast:10 row_mask:0xf bank_mask:0xf\n\t"
                "buffer_load_short_d16_hi v84, v41, %[rm], 0 offen\n\t"
                "v_add_u32 v39, s72, %[kl]\n\t"
                "v_cmp_gt_u32 vcc, s73, v39\n\t"
                "v_add_u32_dpp v41, v54, %[n2] row_newbcast:11 row_mask:0xf bank_mask:0xf\n\t"
                "buffer_load_short_d16 v77, v40, %[rm], 0 offen\n\t"
                "v_add_u32_dpp v40, v54, %[n2] row_newbcast:12 row_mask:0xf bank_mask:0xf\n\t"
                "buffer_load_short_d16_hi v85, v41, %[rm], 0 offen\n\t"
                "v_lshlrev_b32 v39, 3, v39\n\t"
                "s_nop 0\n\t"
                "v_cndmask_b32 v39, 0, v39, vcc\n\t"
                "v_add_u32_dpp v41, v54, %[n2] row_newbcast:13 row_mask:0xf bank_mask:0xf\n\t"
                "buffer_load_short_d16 v78, v40, %[rm], 0 offen\n\t"
                "ds_read_b64 v[52:53], v39\n\t"
                "v_add_u32_dpp v40, v54, %[n2] row_newbcast:14 row_mask:0xf bank_mask:0xf\n\t"
                "buffer_load_short_d16_hi v86, v41, %[rm], 0 offen\n\t"
                "v_readlane_b32 s72, %[desc], s75\n\t"
                "s_add_u32 s75, s75, 1\n\t"
                "s_lshr_b32 s73, s72, 16\n\t"
                "s_and_b32 s72, s72, 0xffff\n\t"
                "v_add_u32_dpp v41, v54, %[n2] row_newbcast:15 row_mask:0xf bank_mask:0xf\n\t"
                "buffer_load_short_d16 v79, v40, %[rm], 0 offen\n\t"
                "buffer_load_short_d16_hi v87, v41, %[rm], 0 offen\n\t"
                "s_waitcnt vmcnt(16)\n\t"
                "v_or_b32 v56, v56, v64\n\t"
                "v_or_b32 v57, v57, v65\n\t"
                "v_or_b32 v58, v58, v66\n\t"
                "v_or_b32 v59, v59, v67\n\t"
                "v_or_b32 v60, v60, v68\n\t"
                "v_or_b32 v61, v61, v69\n\t"
                "v_or_b32 v62, v62, v70\n\t"
                "v_or_b32 v63, v63, v71\n\t"
                "s_waitcnt lgkmcnt(2)\n\t"
                "v_mfma_f32_16x16x32_bf16 v[100:103], v[56:59], v[44:47], v[100:103]\n\t"
                "v_mfma_f32_16x16x32_bf16 v[100:103], v[60:63], v[48:51], v[100:103]\n\t"
                "s_sub_u32 s74, s74, 1\n\t"
                "s_cmp_eq_u32 s74, 0\n\t"
                "s_cbranch_scc1 E4_1_%=\n\t"
                "B3_1_%=:\n\t"
                "s_waitcnt lgkmcnt(0)\n\t"
                "v_add_u32_dpp v40, v52, %[n2] row_newbcast:0 row_mask:0xf bank_mask:0xf\n\t"
                "v_add_u32_dpp v41, v52, %[n2] row_newbcast:1 row_mask:0xf bank_mask:0xf\n\t"
                "buffer_load_short_d16 v56, v40, %[rm], 0 offen\n\t"
                "v_and_b32 v43, 0x3c, v55\n\t"
                "v_mad_u32_u24 v43, v43, 20, %[ww]\n\t"
                "v_add_u32_dpp v40, v52, %[n2] row_newbcast:2 row_mask:0xf bank_mask:0xf\n\t"
                "buffer_load_short_d16_hi v64, v41, %[rm], 0 offen\n\t"
                "v_add_u32_dpp v41, v52, %[n2] row_newbcast:3 row_mask:0xf bank_mask:0xf\n\t"
                "buffer_load_short_d16 v57, v40, %[rm], 0 offen\n\t"
                "s_mov_b64 exec, s[76:77]\n\t"
                "ds_write_b16_d16_hi v43, v55\n\t"
                "s_mov_b64 exec, -1\n\t"
                "v_add_u32_dpp v40, v52, %[n2] row_newbcast:4 row_mask:0xf bank_mask:0xf\n\t"
                "buffer_load_short_d16_hi v65, v41, %[rm], 0 offen\n\t"
                "ds_read_b128 v[44:47], %[wr]\n\t"
                "v_add_u32_dpp v41, v52, %[n2] row_newbcast:5 row_mask:0xf bank_mask:0xf\n\t"
                "buffer_load_short_d16 v58, v40, %[rm], 0 offen\n\t"
                "v_add_u32_dpp v40, v52, %[n2] row_newbcast:6 row_mask:0xf bank_mask:0xf\n\t"
                "buffer_load_short_d16_hi v66, v41, %[rm], 0 offen\n\t"
                "s_mov_b64 exec, s[76:77]\n\t"
                "ds_write_b16 v43, v42\n\t"
                "s_mov_b64 exec, s[78:79]\n\t"
                "ds_write_b16_d16_hi v43, v55\n\t"
                "s_mov_b64 exec, -1\n\t"
                "v_add_u32_dpp v41, v52, %[n2] row_newbcast:7 row_mask:0xf bank_mask:0xf\n\t"
                "buffer_load_short_d16 v59, v40, %[rm], 0 offen\n\t"
                "ds_read_b128 v[48:51], %[wr]\n\t"
                "v_add_u32_dpp v40, v52, %[n2] row_newbcast:8 row_mask:0xf bank_mask:0xf\n\t"
                "buffer_load_short_d16_hi v67, v41, %[rm], 0 offen\n\t"
                "v_add_u32_dpp v41, v52, %[n2] row_newbcast:9 row_mask:0xf bank_mask:0xf\n\t"
                "buffer_load_short_d16 v60, v40, %[rm], 0 offen\n\t"
                "s_mov_b64 exec, s[78:79]\n\t"
                "ds_write_b16 v43, v42\n\t"
                "s_mov_b64 exec, -1\n\t"
                "v_add_u32_dpp v40, v52, %[n2] row_newbcast:10 row_mask:0xf bank_mask:0xf\n\t"
                "buffer_load_short_d16_hi v68, v41, %[rm], 0 offen\n\t"
                "v_add_u32 v39, s72, %[kl]\n\t"
                "v_cmp_gt_u32 vcc, s73, v39\n\t"
                "v_add_u32_dpp v41, v52, %[n2] row_newbcast:11 row_mask:0xf bank_mask:0xf\n\t"
                "buffer_load_short_d16 v61, v40, %[rm], 0 offen\n\t"
                "v_add_u32_dpp v40, v52, %[n2] row_newbcast:12 row_mask:0xf bank_mask:0xf\n\t"
                "buffer_load_short_d16_hi v69, v41, %[rm], 0 offen\n\t"
                "v_lshlrev_b32 v39, 3, v39\n\t"
                "s_nop 0\n\t"
                "v_cndmask_b32 v39, 0, v39, vcc\n\t"
                "v_add_u32_dpp v41, v52, %[n2] row_newbcast:13 row_mask:0xf bank_mask:0xf\n\t"
                "buffer_load_short_d16 v62, v40, %[rm], 0 offen\n\t"
                "ds_read_b64 v[54:55], v39\n\t"
                "v_add_u32_dpp v40, v52, %[n2] row_newbcast:14 row_mask:0xf bank_mask:0xf\n\t"
                "buffer_load_short_d16_hi v70, v41, %[rm], 0 offen\n\t"
                "v_readlane_b32 s72, %[desc], s75\n\t"
                "s_add_u32 s75, s75, 1\n\t"
                "s_lshr_b32 s73, s72, 16\n\t"
                "s_and_b32 s72, s72, 0xffff\n\t"
                "v_add_u32_dpp v41, v52, %[n2] row_newbcast:15 row_mask:0xf bank_mask:0xf\n\t"
                "buffer_load_short_d16 v63, v40, %[rm], 0 offen\n\t"
                "buffer_load_short_d16_hi v71, v41, %[rm], 0 offen\n\t"
                "s_waitcnt vmcnt(16)\n\t"
                "v_or_b32 v72, v72, v80\n\t"
                "v_or_b32 v73, v73, v81\n\t"
                "v_or_b32 v74, v74, v82\n\t"
                "v_or_b32 v75, v75, v83\n\t"
                "v_or_b32 v76, v76, v84\n\t"
                "v_or_b32 v77, v77, v85\n\t"
                "v_or_b32 v78, v78, v86\n\t"
                "v_or_b32 v79, v79, v87\n\t"
                "s_waitcnt lgkmcnt(2)\n\t"
                "v_mfma_f32_16x16x32_bf16 v[100:103], v[72:75], v[44:47], v[100:103]\n\t"
                "v_mfma_f32_16x16x32_bf16 v[100:103], v[76:79], v[48:51], v[100:103]\n\t"
                "s_sub_u32 s74, s74, 1\n\t"
                "s_cmp_eq_u32 s74, 0\n\t"
                "s_cbranch_scc0 B3_0_%=\n\t"
                "s_branch E4_0_%=\n\t"
                "E3_1_%=:\n\t"
                "s_setprio 1\n\t"
                "v_readlane_b32 s74, %[cnt], 3\n\t"
                "s_nop 0\n\t"
                "s_cmp_eq_u32 s74, 0\n\t"
                "s_cbranch_scc0 B3_1_%=\n\t"
                "s_branch E4_1_%=\n\t"
                "E4_0_%=:\n\t"
                "s_setprio 1\n\t"
                "v_readlane_b32 s74, %[cnt], 4\n\t"
                "s_nop 0\n\t"
                "s_cmp_eq_u32 s74, 0\n\t"
                "s_cbranch_scc1 E5_0_%=\n\t"
                "B4_0_%=:\n\t"
                "s_waitcnt lgkmcnt(0)\n\t"
                "v_add_u32_dpp v40, v54, %[n2] row_newbcast:0 row_mask:0xf bank_mask:0xf\n\t"
                "v_add_u32_dpp v41, v54, %[n2] row_newbcast:1 row_mask:0xf bank_mask:0xf\n\t"
                "buffer_load_short_d16 v72, v40, %[rm], 0 offen\n\t"
                "v_and_b32 v43, 0x3c, v53\n\t"
                "v_mad_u32_u24 v43, v43, 20, %[ww]\n\t"
                "v_add_u32_dpp v40, v54, %[n2] row_newbcast:2 row_mask:0xf bank_mask:0xf\n\t"
                "buffer_load_short_d16_hi v80, v41, %[rm], 0 offen\n\t"
                "v_add_u32_dpp v41, v54, %[n2] row_newbcast:3 row_mask:0xf bank_mask:0xf\n\t"
                "buffer_load_short_d16 v73, v40, %[rm], 0 offen\n\t"
                "s_mov_b64 exec, s[76:77]\n\t"
                "ds_write_b16_d16_hi v43, v53\n\t"
                "s_mov_b64 exec, -1\n\t"
                "v_add_u32_dpp v40, v54, %[n2] row_newbcast:4 row_mask:0xf bank_mask:0xf\n\t"
                "buffer_load_short_d16_hi v81, v41, %[rm], 0 offen\n\t"
                "ds_read_b128 v[44:47], %[wr]\n\t"
                "v_add_u32_dpp v41, v54, %[n2] row_newbcast:5 row_mask:0xf bank_mask:0xf\n\t"
                "buffer_load_short_d16 v74, v40, %[rm], 0 offen\n\t"
                "v_add_u32_dpp v40, v54, %[n2] row_newbcast:6 row_mask:0xf bank_mask:0xf\n\t"
                "buffer_load_short_d16_hi v82, v41, %[rm], 0 offen\n\t"
                "s_mov_b64 exec, s[76:77]\n\t"
                "ds_write_b16 v43, v42\n\t"
                "s_mov_b64 exec, s[78:79]\n\t"
                "ds_write_b16_d16_hi v43, v53\n\t"
                "s_mov_b64 exec, -1\n\t"
                "v_add_u32_dpp v41, v54, %[n2] row_newbcast:7 row_mask:0xf bank_mask:0xf\n\t"
                "buffer_load_short_d16 v75, v40, %[rm], 0 offen\n\t"
                "ds_read_b128 v[48:51], %[wr]\n\t"
                "v_add_u32_dpp v40, v54, %[n2] row_newbcast:8 row_mask:0xf bank_mask:0xf\n\t"
                "buffer_load_short_d16_hi v83, v41, %[rm], 0 offen\n\t"
                "v_add_u32_dpp v41, v54, %[n2] row_newbcast:9 row_mask:0xf bank_mask:0xf\n\t"
                "buffer_load_short_d16 v76, v40, %[rm], 0 offen\n\t"
                "s_mov_b64 exec, s[78:79]\n\t"
                "ds_write_b16 v43, v42\n\t"
                "s_mov_b64 exec, -1\n\t"
                "v_add_u32_dpp v40, v54, %[n2] row_newbcast:10 row_mask:0xf bank_mask:0xf\n\t"
                "buffer_load_short_d16_hi v84, v41, %[rm], 0 offen\n\t"
                "v_add_u32 v39, s72, %[kl]\n\t"
                "v_cmp_gt_u32 vcc, s73, v39\n\t"
                "v_add_u32_dpp v41, v54, %[n2] row_newbcast:11 row_mask:0xf bank_mask:0xf\n\t"
                "buffer_load_short_d16 v77, v40, %[rm], 0 offen\n\t"
                "v_add_u32_dpp v40, v54, %[n2] row_newbcast:12 row_mask:0xf bank_mask:0xf\n\t"
                "buffer_load_short_d16_hi v85, v41, %[rm], 0 offen\n\t"
                "v_lshlrev_b32 v39, 3, v39\n\t"
                "s_nop 0\n\t"
                "v_cndmask_b32 v39, 0, v39, vcc\n\t"
                "v_add_u32_dpp v41, v54, %[n2] row_newbcast:13 row_mask:0xf bank_mask:0xf\n\t"
                "buffer_load_short_d16 v78, v40, %[rm], 0 offen\n\t"
                "ds_read_b64 v[52:53], v39\n\t"
                "v_add_u32_dpp v40, v54, %[n2] row_newbcast:14 row_mask:0xf bank_mask:0xf\n\t"
                "buffer_load_short_d16_hi v86, v41, %[rm], 0 offen\n\t"
                "v_readlane_b32 s72, %[desc], s75\n\t"
                "s_add_u32 s75, s75, 1\n\t"
                "s_lshr_b32 s73, s72, 16\n\t"
                "s_and_b32 s72, s72, 0xffff\n\t"
                "v_add_u32_dpp v41, v54, %[n2] row_newbcast:15 row_mask:0xf bank_mask:0xf\n\t"
                "buffer_load_short_d16 v79, v40, %[rm], 0 offen\n\t"
                "buffer_load_short_d16_hi v87, v41, %[rm], 0 offen\n\t"
                "s_waitcnt vmcnt(16)\n\t"
                "v_or_b32 v56, v56, v64\n\t"
                "v_or_b32 v57, v57, v65\n\t"
                "v_or_b32 v58, v58, v66\n\t"
                "v_or_b32 v59, v59, v67\n\t"
                "v_or_b32 v60, v60, v68\n\t"
                "v_or_b32 v61, v61, v69\n\t"
                "v_or_b32 v62, v62, v70\n\t"
                "v_or_b32 v63, v63, v71\n\t"
                "s_waitcnt lgkmcnt(2)\n\t"
                "v_mfma_f32_16x16x32_bf16 v[104:107], v[56:59], v[44:47], v[104:107]\n\t"
                "v_mfma_f32_16x16x32_bf16 v[104:107], v[60:63], v[48:51], v[104:107]\n\t"
                "s_sub_u32 s74, s74, 1\n\t"
                "s_cmp_eq_u32 s74, 0\n\t"
                "s_cbranch_scc1 E5_1_%=\n\t"
                "B4_1_%=:\n\t"
                "s_waitcnt lgkmcnt(0)\n\t"
                "v_add_u32_dpp v40, v52, %[n2] row_newbcast:0 row_mask:0xf bank_mask:0xf\n\t"
                "v_add_u32_dpp v41, v52, %[n2] row_newbcast:1 row_mask:0xf bank_mask:0xf\n\t"
                "buffer_load_short_d16 v56, v40, %[rm], 0 offen\n\t"
                "v_and_b32 v43, 0x3c, v55\n\t"
                "v_mad_u32_u24 v43, v43, 20, %[ww]\n\t"
                "v_add_u32_dpp v40, v52, %[n2] row_newbcast:2 row_mask:0xf bank_mask:0xf\n\t"
                "buffer_load_short_d16_hi v64, v41, %[rm], 0 offen\n\t"
                "v_add_u32_dpp v41, v52, %[n2] row_newbcast:3 row_mask:0xf bank_mask:0xf\n\t"
                "buffer_load_short_d16 v57, v40, %[rm], 0 offen\n\t"
                "s_mov_b64 exec, s[76:77]\n\t"
                "ds_write_b16_d16_hi v43, v55\n\t"
                "s_mov_b64 exec, -1\n\t"
                "v_add_u32_dpp v40, v52, %[n2] row_newbcast:4 row_mask:0xf bank_mask:0xf\n\t"
                "buffer_load_short_d16_hi v65, v41, %[rm], 0 offen\n\t"
                "ds_read_b128 v[44:47], %[wr]\n\t"
                "v_add_u32_dpp v41, v52, %[n2] row_newbcast:5 row_mask:0xf bank_mask:0xf\n\t"
                "buffer_load_short_d16 v58, v40, %[rm], 0 offen\n\t"
                "v_add_u32_dpp v40, v52, %[n2] row_newbcast:6 row_mask:0xf bank_mask:0xf\n\t"
                "buffer_load_short_d16_hi v66, v41, %[rm], 0 offen\n\t"
                "s_mov_b64 exec, s[76:77]\n\t"
                "ds_write_b16 v43, v42\n\t"
                "s_mov_b64 exec, s[78:79]\n\t"
                "ds_write_b16_d16_hi v43, v55\n\t"
                "s_mov_b64 exec, -1\n\t"
                "v_add_u32_dpp v41, v52, %[n2] row_newbcast:7 row_mask:0xf bank_mask:0xf\n\t"
                "buffer_load_short_d16 v59, v40, %[rm], 0 offen\n\t"
                "ds_read_b128 v[48:51], %[wr]\n\t"
                "v_add_u32_dpp v40, v52, %[n2] row_newbcast:8 row_mask:0xf bank_mask:0xf\n\t"
                "buffer_load_short_d16_hi v67, v41, %[rm], 0 offen\n\t"
                "v_add_u32_dpp v41, v52, %[n2] row_newbcast:9 row_mask:0xf bank_mask:0xf\n\t"
                "buffer_load_short_d16 v60, v40, %[rm], 0 offen\n\t"
                "s_mov_b64 exec, s[78:79]\n\t"
                "ds_write_b16 v43, v42\n\t"
                "s_mov_b64 exec, -1\n\t"
                "v_add_u32_dpp v40, v52, %[n2] row_newbcast:10 row_mask:0xf bank_mask:0xf\n\t"
                "buffer_load_short_d16_hi v68, v41, %[rm], 0 offen\n\t"
                "v_add_u32 v39, s72, %[kl]\n\t"
                "v_cmp_gt_u32 vcc, s73, v39\n\t"
                "v_add_u32_dpp v41, v52, %[n2] row_newbcast:11 row_mask:0xf bank_mask:0xf\n\t"
                "buffer_load_short_d16 v61, v40, %[rm], 0 offen\n\t"
                "v_add_u32_dpp v40, v52, %[n2] row_newbcast:12 row_mask:0xf bank_mask:0xf\n\t"
                "buffer_load_short_d16_hi v69, v41, %[rm], 0 offen\n\t"
                "v_lshlrev_b32 v39, 3, v39\n\t"
                "s_nop 0\n\t"
                "v_cndmask_b32 v39, 0, v39, vcc\n\t"
                "v_add_u32_dpp v41, v52, %[n2] row_newbcast:13 row_mask:0xf bank_mask:0xf\n\t"
                "buffer_load_short_d16 v62, v40, %[rm], 0 offen\n\t"
                "ds_read_b64 v[54:55], v39\n\t"
                "v_add_u32_dpp v40, v52, %[n2] row_newbcast:14 row_mask:0xf bank_mask:0xf\n\t"
                "buffer_load_short_d16_hi v70, v41, %[rm], 0 offen\n\t"
                "v_readlane_b32 s72, %[desc], s75\n\t"
                "s_add_u32 s75, s75, 1\n\t"
                "s_lshr_b32 s73, s72, 16\n\t"
                "s_and_b32 s72, s72, 0xffff\n\t"
                "v_add_u32_dpp v41, v52, %[n2] row_newbcast:15 row_mask:0xf bank_mask:0xf\n\t"
                "buffer_load_short_d16 v63, v40, %[rm], 0 offen\n\t"
                "buffer_load_short_d16_hi v71, v41, %[rm], 0 offen\n\t"
                "s_waitcnt vmcnt(16)\n\t"
                "v_or_b32 v72, v72, v80\n\t"
                "v_or_b32 v73, v73, v81\n\t"
                "v_or_b32 v74, v74, v82\n\t"
                "v_or_b32 v75, v75, v83\n\t"
                "v_or_b32 v76, v76, v84\n\t"
                "v_or_b32 v77, v77, v85\n\t"
                "v_or_b32 v78, v78, v86\n\t"
                "v_or_b32 v79, v79, v87\n\t"
                "s_waitcnt lgkmcnt(2)\n\t"
                "v_mfma_f32_16x16x32_bf16 v[104:107], v[72:75], v[44:47], v[104:107]\n\t"
                "v_mfma_f32_16x16x32_bf16 v[104:107], v[76:79], v[48:51], v[104:107]\n\t"
                "s_sub_u32 s74, s74, 1\n\t"
                "s_cmp_eq_u32 s74, 0\n\t"
                "s_cbranch_scc0 B4_0_%=\n\t"
                "s_branch E5_0_%=\n\t"
                "E4_1_%=:\n\t"
                "s_setprio 1\n\t"
                "v_readlane_b32 s74, %[cnt], 4\n\t"
                "s_nop 0\n\t"
                "s_cmp_eq_u32 s74, 0\n\t"
                "s_cbranch_scc0 B4_1_%=\n\t"
                "s_branch E5_1_%=\n\t"
                "E5_0_%=:\n\t"
                "s_setprio 0\n\t"
                "v_readlane_b32 s74, %[cnt], 5\n\t"
                "s_nop 0\n\t"
                "s_cmp_eq_u32 s74, 0\n\t"
                "s_cbranch_scc1 E6_0_%=\n\t"
                "B5_0_%=:\n\t"
                "s_waitcnt lgkmcnt(0)\n\t"
                "v_add_u32_dpp v40, v54, %[n2] row_newbcast:0 row_mask:0xf bank_mask:0xf\n\t"
                "v_add_u32_dpp v41, v54, %[n2] row_newbcast:1 row_mask:0xf bank_mask:0xf\n\t"
                "buffer_load_short_d16 v72, v40, %[rm], 0 offen\n\t"
                "v_and_b32 v43, 0x3c, v53\n\t"
                "v_mad_u32_u24 v43, v43, 20, %[ww]\n\t"
                "v_add_u32_dpp v40, v54, %[n2] row_newbcast:2 row_mask:0xf bank_mask:0xf\n\t"
                "buffer_load_short_d16_hi v80, v41, %[rm], 0 offen\n\t"
                "v_add_u32_dpp v41, v54, %[n2] row_newbcast:3 row_mask:0xf bank_mask:0xf\n\t"
                "buffer_load_short_d16 v73, v40, %[rm], 0 offen\n\t"
                "s_mov_b64 exec, s[76:77]\n\t"
                "ds_write_b16_d16_hi v43, v53\n\t"
                "s_mov_b64 exec, -1\n\t"
                "v_add_u32_dpp v40, v54, %[n2] row_newbcast:4 row_mask:0xf bank_mask:0xf\n\t"
                "buffer_load_short_d16_hi v81, v41, %[rm], 0 offen\n\t"
                "ds_read_b128 v[44:47], %[wr]\n\t"
                "v_add_u32_dpp v41, v54, %[n2] row_newbcast:5 row_mask:0xf bank_mask:0xf\n\t"
                "buffer_load_short_d16 v74, v40, %[rm], 0 offen\n\t"
                "v_add_u32_dpp v40, v54, %[n2] row_newbcast:6 row_mask:0xf bank_mask:0xf\n\t"
                "buffer_load_short_d16_hi v82, v41, %[rm], 0 offen\n\t"
                "s_mov_b64 exec, s[76:77]\n\t"
                "ds_write_b16 v43, v42\n\t"
                "s_mov_b64 exec, s[78:79]\n\t"
                "ds_write_b16_d16_hi v43, v53\n\t"
                "s_mov_b64 exec, -1\n\t"
                "v_add_u32_dpp v41, v54, %[n2] row_newbcast:7 row_mask:0xf bank_mask:0xf\n\t"
                "buffer_load_short_d16 v75, v40, %[rm], 0 offen\n\t"
                "ds_read_b128 v[48:51], %[wr]\n\t"
                "v_add_u32_dpp v40, v54, %[n2] row_newbcast:8 row_mask:0xf bank_mask:0xf\n\t"
                "buffer_load_short_d16_hi v83, v41, %[rm], 0 offen\n\t"
                "v_add_u32_dpp v41, v54, %[n2] row_newbcast:9 row_mask:0xf bank_mask:0xf\n\t"
                "buffer_load_short_d16 v76, v40, %[rm], 0 offen\n\t"
                "s_mov_b64 exec, s[78:79]\n\t"
                "ds_write_b16 v43, v42\n\t"
                "s_mov_b64 exec, -1\n\t"
                "v_add_u32_dpp v40, v54, %[n2] row_newbcast:10 row_mask:0xf bank_mask:0xf\n\t"
                "buffer_load_short_d16_hi v84, v41, %[rm], 0 offen\n\t"
                "v_add_u32 v39, s72, %[kl]\n\t"
                "v_cmp_gt_u32 vcc, s73, v39\n\t"
                "v_add_u32_dpp v41, v54, %[n2] row_newbcast:11 row_mask:0xf bank_mask:0xf\n\t"
                "buffer_load_short_d16 v77, v40, %[rm], 0 offen\n\t"
                "v_add_u32_dpp v40, v54, %[n2] row_newbcast:12 row_mask:0xf bank_mask:0xf\n\t"
                "buffer_load_short_d16_hi v85, v41, %[rm], 0 offen\n\t"
                "v_lshlrev_b32 v39, 3, v39\n\t"
                "s_nop 0\n\t"
                "v_cndmask_b32 v39, 0, v39, vcc\n\t"
                "v_add_u32_dpp v41, v54, %[n2] row_newbcast:13 row_mask:0xf bank_mask:0xf\n\t"
                "buffer_load_short_d16 v78, v40, %[rm], 0 offen\n\t"
                "ds_read_b64 v[52:53], v39\n\t"
                "v_add_u32_dpp v40, v54, %[n2] row_newbcast:14 row_mask:0xf bank_mask:0xf\n\t"
                "buffer_load_short_d16_hi v86, v41, %[rm], 0 offen\n\t"
                "v_readlane_b32 s72, %[desc], s75\n\t"
                "s_add_u32 s75, s75, 1\n\t"
                "s_lshr_b32 s73, s72, 16\n\t"
                "s_and_b32 s72, s72, 0xffff\n\t"
                "v_add_u32_dpp v41, v54, %[n2] row_newbcast:15 row_mask:0xf bank_mask:0xf\n\t"
                "buffer_load_short_d16 v79, v40, %[rm], 0 offen\n\t"
                "buffer_load_short_d16_hi v87, v41, %[rm], 0 offen\n\t"
                "s_waitcnt vmcnt(16)\n\t"
                "v_or_b32 v56, v56, v64\n\t"
                "v_or_b32 v57, v57, v65\n\t"
                "v_or_b32 v58, v58, v66\n\t"
                "v_or_b32 v59, v59, v67\n\t"
                "v_or_b32 v60, v60, v68\n\t"
                "v_or_b32 v61, v61, v69\n\t"
                "v_or_b32 v62, v62, v70\n\t"
                "v_or_b32 v63, v63, v71\n\t"
                "s_waitcnt lgkmcnt(2)\n\t"
                "v_mfma_f32_16x16x32_bf16 v[108:111], v[56:59], v[44:47], v[108:111]\n\t"
                "v_mfma_f32_16x16x32_bf16 v[108:111], v[60:63], v[48:51], v[108:111]\n\t"
                "s_sub_u32 s74, s74, 1\n\t"
                "s_cmp_eq_u32 s74, 0\n\t"
                "s_cbranch_scc1 E6_1_%=\n\t"
                "B5_1_%=:\n\t"
                "s_waitcnt lgkmcnt(0)\n\t"
                "v_add_u32_dpp v40, v52, %[n2] row_newbcast:0 row_mask:0xf bank_mask:0xf\n\t"
                "v_add_u32_dpp v41, v52, %[n2] row_newbcast:1 row_mask:0xf bank_mask:0xf\n\t"
                "buffer_load_short_d16 v56, v40, %[rm], 0 offen\n\t"
                "v_and_b32 v43, 0x3c, v55\n\t"
                "v_mad_u32_u24 v43, v43, 20, %[ww]\n\t"
                "v_add_u32_dpp v40, v52, %[n2] row_newbcast:2 row_mask:0xf bank_mask:0xf\n\t"
                "buffer_load_short_d16_hi v64, v41, %[rm], 0 offen\n\t"
                "v_add_u32_dpp v41, v52, %[n2] row_newbcast:3 row_mask:0xf bank_mask:0xf\n\t"
                "buffer_load_short_d16 v57, v40, %[rm], 0 offen\n\t"
                "s_mov_b64 exec, s[76:77]\n\t"
                "ds_write_b16_d16_hi v43, v55\n\t"
                "s_mov_b64 exec, -1\n\t"
                "v_add_u32_dpp v40, v52, %[n2] row_newbcast:4 row_mask:0xf bank_mask:0xf\n\t"
                "buffer_load_short_d16_hi v65, v41, %[rm], 0 offen\n\t"
                "ds_read_b128 v[44:47], %[wr]\n\t"
                "v_add_u32_dpp v41, v52, %[n2] row_newbcast:5 row_mask:0xf bank_mask:0xf\n\t"
                "buffer_load_short_d16 v58, v40, %[rm], 0 offen\n\t"
                "v_add_u32_dpp v40, v52, %[n2] row_newbcast:6 row_mask:0xf bank_mask:0xf\n\t"
                "buffer_load_short_d16_hi v66, v41, %[rm], 0 offen\n\t"
                "s_mov_b64 exec, s[76:77]\n\t"
                "ds_write_b16 v43, v42\n\t"
                "s_mov_b64 exec, s[78:79]\n\t"
                "ds_write_b16_d16_hi v43, v55\n\t"
                "s_mov_b64 exec, -1\n\t"
                "v_add_u32_dpp v41, v52, %[n2] row_newbcast:7 row_mask:0xf bank_mask:0xf\n\t"
                "buffer_load_short_d16 v59, v40, %[rm], 0 offen\n\t"
                "ds_read_b128 v[48:51], %[wr]\n\t"
                "v_add_u32_dpp v40, v52, %[n2] row_newbcast:8 row_mask:0xf bank_mask:0xf\n\t"
                "buffer_load_short_d16_hi v67, v41, %[rm], 0 offen\n\t"
                "v_add_u32_dpp v41, v52, %[n2] row_newbcast:9 row_mask:0xf bank_mask:0xf\n\t"
                "buffer_load_short_d16 v60, v40, %[rm], 0 offen\n\t"
                "s_mov_b64 exec, s[78:79]\n\t"
                "ds_write_b16 v43, v42\n\t"
                "s_mov_b64 exec, -1\n\t"
                "v_add_u32_dpp v40, v52, %[n2] row_newbcast:10 row_mask:0xf bank_mask:0xf\n\t"
                "buffer_load_short_d16_hi v68, v41, %[rm], 0 offen\n\t"
                "v_add_u32 v39, s72, %[kl]\n\t"
                "v_cmp_gt_u32 vcc, s73, v39\n\t"
                "v_add_u32_dpp v41, v52, %[n2] row_newbcast:11 row_mask:0xf bank_mask:0xf\n\t"
                "buffer_load_short_d16 v61, v40, %[rm], 0 offen\n\t"
                "v_add_u32_dpp v40, v52, %[n2] row_newbcast:12 row_mask:0xf bank_mask:0xf\n\t"
                "buffer_load_short_d16_hi v69, v41, %[rm], 0 offen\n\t"
                "v_lshlrev_b32 v39, 3, v39\n\t"
                "s_nop 0\n\t"
                "v_cndmask_b32 v39, 0, v39, vcc\n\t"
                "v_add_u32_dpp v41, v52, %[n2] row_newbcast:13 row_mask:0xf bank_mask:0xf\n\t"
                "buffer_load_short_d16 v62, v40, %[rm], 0 offen\n\t"
                "ds_read_b64 v[54:55], v39\n\t"
                "v_add_u32_dpp v40, v52, %[n2] row_newbcast:14 row_mask:0xf bank_mask:0xf\n\t"
                "buffer_load_short_d16_hi v70, v41, %[rm], 0 offen\n\t"
                "v_readlane_b32 s72, %[desc], s75\n\t"
                "s_add_u32 s75, s75, 1\n\t"
                "s_lshr_b32 s73, s72, 16\n\t"
                "s_and_b32 s72, s72, 0xffff\n\t"
                "v_add_u32_dpp v41, v52, %[n2] row_newbcast:15 row_mask:0xf bank_mask:0xf\n\t"
                "buffer_load_short_d16 v63, v40, %[rm], 0 offen\n\t"
                "buffer_load_short_d16_hi v71, v41, %[rm], 0 offen\n\t"
                "s_waitcnt vmcnt(16)\n\t"
                "v_or_b32 v72, v72, v80\n\t"
                "v_or_b32 v73, v73, v81\n\t"
                "v_or_b32 v74, v74, v82\n\t"
                "v_or_b32 v75, v75, v83\n\t"
                "v_or_b32 v76, v76, v84\n\t"
                "v_or_b32 v77, v77, v85\n\t"
                "v_or_b32 v78, v78, v86\n\t"
                "v_or_b32 v79, v79, v87\n\t"
                "s_waitcnt lgkmcnt(2)\n\t"
                "v_mfma_f32_16x16x32_bf16 v[108:111], v[72:75], v[44:47], v[108:111]\n\t"
                "v_mfma_f32_16x16x32_bf16 v[108:111], v[76:79], v[48:51], v[108:111]\n\t"
                "s_sub_u32 s74, s74, 1\n\t"
                "s_cmp_eq_u32 s74, 0\n\t"
                "s_cbranch_scc0 B5_0_%=\n\t"
                "s_branch E6_0_%=\n\t"
                "E5_1_%=:\n\t"
                "s_setprio 0\n\t"
                "v_readlane_b32 s74, %[cnt], 5\n\t"
                "s_nop 0\n\t"
                "s_cmp_eq_u32 s74, 0\n\t"
                "s_cbranch_scc0 B5_1_%=\n\t"
                "s_branch E6_1_%=\n\t"
                "E6_0_%=:\n\t"
                "s_setprio 0\n\t"
                "v_readlane_b32 s74, %[cnt], 6\n\t"
                "s_nop 0\n\t"
                "s_cmp_eq_u32 s74, 0\n\t"
                "s_cbranch_scc1 E7_0_%=\n\t"
                "B6_0_%=:\n\t"
                "s_waitcnt lgkmcnt(0)\n\t"
                "v_add_u32_dpp v40, v54, %[n2] row_newbcast:0 row_mask:0xf bank_mask:0xf\n\t"
                "v_add_u32_dpp v41, v54, %[n2] row_newbcast:1 row_mask:0xf bank_mask:0xf\n\t"
                "buffer_load_short_d16 v72, v40, %[rm], 0 offen\n\t"
                "v_and_b32 v43, 0x3c, v53\n\t"
                "v_mad_u32_u24 v43, v43, 20, %[ww]\n\t"
                "v_add_u32_dpp v40, v54, %[n2] row_newbcast:2 row_mask:0xf bank_mask:0xf\n\t"
                "buffer_load_short_d16_hi v80, v41, %[rm], 0 offen\n\t"
                "v_add_u32_dpp v41, v54, %[n2] row_newbcast:3 row_mask:0xf bank_mask:0xf\n\t"
                "buffer_load_short_d16 v73, v40, %[rm], 0 offen\n\t"
                "s_mov_b64 exec, s[76:77]\n\t"
                "ds_write_b16_d16_hi v43, v53\n\t"
                "s_mov_b64 exec, -1\n\t"
                "v_add_u32_dpp v40, v54, %[n2] row_newbcast:4 row_mask:0xf bank_mask:0xf\n\t"
                "buffer_load_short_d16_hi v81, v41, %[rm], 0 offen\n\t"
                "ds_read_b128 v[44:47], %[wr]\n\t"
                "v_add_u32_dpp v41, v54, %[n2] row_newbcast:5 row_mask:0xf bank_mask:0xf\n\t"
                "buffer_load_short_d16 v74, v40, %[rm], 0 offen\n\t"
                "v_add_u32_dpp v40, v54, %[n2] row_newbcast:6 row_mask:0xf bank_mask:0xf\n\t"
                "buffer_load_short_d16_hi v82, v41, %[rm], 0 offen\n\t"
                "s_mov_b64 exec, s[76:77]\n\t"
                "ds_write_b16 v43, v42\n\t"
                "s_mov_b64 exec, s[78:79]\n\t"
                "ds_write_b16_d16_hi v43, v53\n\t"
                "s_mov_b64 exec, -1\n\t"
                "v_add_u32_dpp v41, v54, %[n2] row_newbcast:7 row_mask:0xf bank_mask:0xf\n\t"
                "buffer_load_short_d16 v75, v40, %[rm], 0 offen\n\t"
                "ds_read_b128 v[48:51], %[wr]\n\t"
                "v_add_u32_dpp v40, v54, %[n2] row_newbcast:8 row_mask:0xf bank_mask:0xf\n\t"
                "buffer_load_short_d16_hi v83, v41, %[rm], 0 offen\n\t"
                "v_add_u32_dpp v41, v54, %[n2] row_newbcast:9 row_mask:0xf bank_mask:0xf\n\t"
                "buffer_load_short_d16 v76, v40, %[rm], 0 offen\n\t"
                "s_mov_b64 exec, s[78:79]\n\t"
                "ds_write_b16 v43, v42\n\t"
                "s_mov_b64 exec, -1\n\t"
                "v_add_u32_dpp v40, v54, %[n2] row_newbcast:10 row_mask:0xf bank_mask:0xf\n\t"
                "buffer_load_short_d16_hi v84, v41, %[rm], 0 offen\n\t"
                "v_add_u32 v39, s72, %[kl]\n\t"
                "v_cmp_gt_u32 vcc, s73, v39\n\t"
                "v_add_u32_dpp v41, v54, %[n2] row_newbcast:11 row_mask:0xf bank_mask:0xf\n\t"
                "buffer_load_short_d16 v77, v40, %[rm], 0 offen\n\t"
                "v_add_u32_dpp v40, v54, %[n2] row_newbcast:12 row_mask:0xf bank_mask:0xf\n\t"
                "buffer_load_short_d16_hi v85, v41, %[rm], 0 offen\n\t"
                "v_lshlrev_b32 v39, 3, v39\n\t"
                "s_nop 0\n\t"
                "v_cndmask_b32 v39, 0, v39, vcc\n\t"
                "v_add_u32_dpp v41, v54, %[n2] row_newbcast:13 row_mask:0xf bank_mask:0xf\n\t"
                "buffer_load_short_d16 v78, v40, %[rm], 0 offen\n\t"
                "ds_read_b64 v[52:53], v39\n\t"
                "v_add_u32_dpp v40, v54, %[n2] row_newbcast:14 row_mask:0xf bank_mask:0xf\n\t"
                "buffer_load_short_d16_hi v86, v41, %[rm], 0 offen\n\t"
                "v_readlane_b32 s72, %[desc], s75\n\t"
                "s_add_u32 s75, s75, 1\n\t"
                "s_lshr_b32 s73, s72, 16\n\t"
                "s_and_b32 s72, s72, 0xffff\n\t"
                "v_add_u32_dpp v41, v54, %[n2] row_newbcast:15 row_mask:0xf bank_mask:0xf\n\t"
                "buffer_load_short_d16 v79, v40, %[rm], 0 offen\n\t"
                "buffer_load_short_d16_hi v87, v41, %[rm], 0 offen\n\t"
                "s_waitcnt vmcnt(16)\n\t"
                "v_or_b32 v56, v56, v64\n\t"
                "v_or_b32 v57, v57, v65\n\t"
                "v_or_b32 v58, v58, v66\n\t"
                "v_or_b32 v59, v59, v67\n\t"
                "v_or_b32 v60, v60, v68\n\t"
                "v_or_b32 v61, v61, v69\n\t"
                "v_or_b32 v62, v62, v70\n\t"
                "v_or_b32 v63, v63, v71\n\t"
                "s_waitcnt lgkmcnt(2)\n\t"
                "v_mfma_f32_16x16x32_bf16 v[112:115], v[56:59], v[44:47], v[112:115]\n\t"
                "v_mfma_f32_16x16x32_bf16 v[112:115], v[60:63], v[48:51], v[112:115]\n\t"
                "s_sub_u32 s74, s74, 1\n\t"
                "s_cmp_eq_u32 s74, 0\n\t"
                "s_cbranch_scc1 E7_1_%=\n\t"
                "B6_1_%=:\n\t"
                "s_waitcnt lgkmcnt(0)\n\t"
                "v_add_u32_dpp v40, v52, %[n2] row_newbcast:0 row_mask:0xf bank_mask:0xf\n\t"
                "v_add_u32_dpp v41, v52, %[n2] row_newbcast:1 row_mask:0xf bank_mask:0xf\n\t"
                "buffer_load_short_d16 v56, v40, %[rm], 0 offen\n\t"
                "v_and_b32 v43, 0x3c, v55\n\t"
                "v_mad_u32_u24 v43, v43, 20, %[ww]\n\t"
                "v_add_u32_dpp v40, v52, %[n2] row_newbcast:2 row_mask:0xf bank_mask:0xf\n\t"
                "buffer_load_short_d16_hi v64, v41, %[rm], 0 offen\n\t"
                "v_add_u32_dpp v41, v52, %[n2] row_newbcast:3 row_mask:0xf bank_mask:0xf\n\t"
                "buffer_load_short_d16 v57, v40, %[rm], 0 offen\n\t"
                "s_mov_b64 exec, s[76:77]\n\t"
                "ds_write_b16_d16_hi v43, v55\n\t"
                "s_mov_b64 exec, -1\n\t"
                "v_add_u32_dpp v40, v52, %[n2] row_newbcast:4 row_mask:0xf bank_mask:0xf\n\t"
                "buffer_load_short_d16_hi v65, v41, %[rm], 0 offen\n\t"
                "ds_read_b128 v[44:47], %[wr]\n\t"
                "v_add_u32_dpp v41, v52, %[n2] row_newbcast:5 row_mask:0xf bank_mask:0xf\n\t"
                "buffer_load_short_d16 v58, v40, %[rm], 0 offen\n\t"
                "v_add_u32_dpp v40, v52, %[n2] row_newbcast:6 row_mask:0xf bank_mask:0xf\n\t"
                "buffer_load_short_d16_hi v66, v41, %[rm], 0 offen\n\t"
                "s_mov_b64 exec, s[76:77]\n\t"
                "ds_write_b16 v43, v42\n\t"
                "s_mov_b64 exec, s[78:79]\n\t"
                "ds_write_b16_d16_hi v43, v55\n\t"
                "s_mov_b64 exec, -1\n\t"
                "v_add_u32_dpp v41, v52, %[n2] row_newbcast:7 row_mask:0xf bank_mask:0xf\n\t"
                "buffer_load_short_d16 v59, v40, %[rm], 0 offen\n\t"
                "ds_read_b128 v[48:51], %[wr]\n\t"
                "v_add_u32_dpp v40, v52, %[n2] row_newbcast:8 row_mask:0xf bank_mask:0xf\n\t"
                "buffer_load_short_d16_hi v67, v41, %[rm], 0 offen\n\t"
                "v_add_u32_dpp v41, v52, %[n2] row_newbcast:9 row_mask:0xf bank_mask:0xf\n\t"
                "buffer_load_short_d16 v60, v40, %[rm], 0 offen\n\t"
                "s_mov_b64 exec, s[78:79]\n\t"
                "ds_write_b16 v43, v42\n\t"
                "s_mov_b64 exec, -1\n\t"
                "v_add_u32_dpp v40, v52, %[n2] row_newbcast:10 row_mask:0xf bank_mask:0xf\n\t"
                "buffer_load_short_d16_hi v68, v41, %[rm], 0 offen\n\t"
                "v_add_u32 v39, s72, %[kl]\n\t"
                "v_cmp_gt_u32 vcc, s73, v39\n\t"
                "v_add_u32_dpp v41, v52, %[n2] row_newbcast:11 row_mask:0xf bank_mask:0xf\n\t"
                "buffer_load_short_d16 v61, v40, %[rm], 0 offen\n\t"
                "v_add_u32_dpp v40, v52, %[n2] row_newbcast:12 row_mask:0xf bank_mask:0xf\n\t"
                "buffer_load_short_d16_hi v69, v41, %[rm], 0 offen\n\t"
                "v_lshlrev_b32 v39, 3, v39\n\t"
                "s_nop 0\n\t"
                "v_cndmask_b32 v39, 0, v39, vcc\n\t"
                "v_add_u32_dpp v41, v52, %[n2] row_newbcast:13 row_mask:0xf bank_mask:0xf\n\t"
                "buffer_load_short_d16 v62, v40, %[rm], 0 offen\n\t"
                "ds_read_b64 v[54:55], v39\n\t"
                "v_add_u32_dpp v40, v52, %[n2] row_newbcast:14 row_mask:0xf bank_mask:0xf\n\t"
                "buffer_load_short_d16_hi v70, v41, %[rm], 0 offen\n\t"
                "v_readlane_b32 s72, %[desc], s75\n\t"
                "s_add_u32 s75, s75, 1\n\t"
                "s_lshr_b32 s73, s72, 16\n\t"
                "s_and_b32 s72, s72, 0xffff\n\t"
                "v_add_u32_dpp v41, v52, %[n2] row_newbcast:15 row_mask:0xf bank_mask:0xf\n\t"
                "buffer_load_short_d16 v63, v40, %[rm], 0 offen\n\t"
                "buffer_load_short_d16_hi v71, v41, %[rm], 0 offen\n\t"
                "s_waitcnt vmcnt(16)\n\t"
                "v_or_b32 v72, v72, v80\n\t"
                "v_or_b32 v73, v73, v81\n\t"
                "v_or_b32 v74, v74, v82\n\t"
                "v_or_b32 v75, v75, v83\n\t"
                "v_or_b32 v76, v76, v84\n\t"
                "v_or_b32 v77, v77, v85\n\t"
                "v_or_b32 v78, v78, v86\n\t"
                "v_or_b32 v79, v79, v87\n\t"
                "s_waitcnt lgkmcnt(2)\n\t"
                "v_mfma_f32_16x16x32_bf16 v[112:115], v[72:75], v[44:47], v[112:115]\n\t"
                "v_mfma_f32_16x16x32_bf16 v[112:115], v[76:79], v[48:51], v[112:115]\n\t"
                "s_sub_u32 s74, s74, 1\n\t"
                "s_cmp_eq_u32 s74, 0\n\t"
                "s_cbranch_scc0 B6_0_%=\n\t"
                "s_branch E7_0_%=\n\t"
                "E6_1_%=:\n\t"
                "s_setprio 0\n\t"
                "v_readlane_b32 s74, %[cnt], 6\n\t"
                "s_nop 0\n\t"
                "s_cmp_eq_u32 s74, 0\n\t"
                "s_cbranch_scc0 B6_1_%=\n\t"
                "s_branch E7_1_%=\n\t"
                "E7_0_%=:\n\t"
                "s_setprio 0\n\t"
                "v_readlane_b32 s74, %[cnt], 7\n\t"
                "s_nop 0\n\t"
                "s_cmp_eq_u32 s74, 0\n\t"
                "s_cbranch_scc1 E8_0_%=\n\t"
                "B7_0_%=:\n\t"
                "s_waitcnt lgkmcnt(0)\n\t"
                "v_add_u32_dpp v40, v54, %[n2] row_newbcast:0 row_mask:0xf bank_mask:0xf\n\t"
                "v_add_u32_dpp v41, v54, %[n2] row_newbcast:1 row_mask:0xf bank_mask:0xf\n\t"
                "buffer_load_short_d16 v72, v40, %[rm], 0 offen\n\t"
                "v_and_b32 v43, 0x3c, v53\n\t"
                "v_mad_u32_u24 v43, v43, 20, %[ww]\n\t"
                "v_add_u32_dpp v40, v54, %[n2] row_newbcast:2 row_mask:0xf bank_mask:0xf\n\t"
                "buffer_load_short_d16_hi v80, v41, %[rm], 0 offen\n\t"
                "v_add_u32_dpp v41, v54, %[n2] row_newbcast:3 row_mask:0xf bank_mask:0xf\n\t"
                "buffer_load_short_d16 v73, v40, %[rm], 0 offen\n\t"
                "s_mov_b64 exec, s[76:77]\n\t"
                "ds_write_b16_d16_hi v43, v53\n\t"
                "s_mov_b64 exec, -1\n\t"
                "v_add_u32_dpp v40, v54, %[n2] row_newbcast:4 row_mask:0xf bank_mask:0xf\n\t"
                "buffer_load_short_d16_hi v81, v41, %[rm], 0 offen\n\t"
                "ds_read_b128 v[44:47], %[wr]\n\t"
                "v_add_u32_dpp v41, v54, %[n2] row_newbcast:5 row_mask:0xf bank_mask:0xf\n\t"
                "buffer_load_short_d16 v74, v40, %[rm], 0 offen\n\t"
                "v_add_u32_dpp v40, v54, %[n2] row_newbcast:6 row_mask:0xf bank_mask:0xf\n\t"
                "buffer_load_short_d16_hi v82, v41, %[rm], 0 offen\n\t"
                "s_mov_b64 exec, s[76:77]\n\t"
                "ds_write_b16 v43, v42\n\t"
                "s_mov_b64 exec, s[78:79]\n\t"
                "ds_write_b16_d16_hi v43, v53\n\t"
                "s_mov_b64 exec, -1\n\t"
                "v_add_u32_dpp v41, v54, %[n2] row_newbcast:7 row_mask:0xf bank_mask:0xf\n\t"
                "buffer_load_short_d16 v75, v40, %[rm], 0 offen\n\t"
                "ds_read_b128 v[48:51], %[wr]\n\t"
                "v_add_u32_dpp v40, v54, %[n2] row_newbcast:8 row_mask:0xf bank_mask:0xf\n\t"
                "buffer_load_short_d16_hi v83, v41, %[rm], 0 offen\n\t"
                "v_add_u32_dpp v41, v54, %[n2] row_newbcast:9 row_mask:0xf bank_mask:0xf\n\t"
                "buffer_load_short_d16 v76, v40, %[rm], 0 offen\n\t"
                "s_mov_b64 exec, s[78:79]\n\t"
                "ds_write_b16 v43, v42\n\t"
                "s_mov_b64 exec, -1\n\t"
                "v_add_u32_dpp v40, v54, %[n2] row_newbcast:10 row_mask:0xf bank_mask:0xf\n\t"
                "buffer_load_short_d16_hi v84, v41, %[rm], 0 offen\n\t"
                "v_add_u32 v39, s72, %[kl]\n\t"
                "v_cmp_gt_u32 vcc, s73, v39\n\t"
                "v_add_u32_dpp v41, v54, %[n2] row_newbcast:11 row_mask:0xf bank_mask:0xf\n\t"
                "buffer_load_short_d16 v77, v40, %[rm], 0 offen\n\t"
                "v_add_u32_dpp v40, v54, %[n2] row_newbcast:12 row_mask:0xf bank_mask:0xf\n\t"
                "buffer_load_short_d16_hi v85, v41, %[rm], 0 offen\n\t"
                "v_lshlrev_b32 v39, 3, v39\n\t"
                "s_nop 0\n\t"
                "v_cndmask_b32 v39, 0, v39, vcc\n\t"
                "v_add_u32_dpp v41, v54, %[n2] row_newbcast:13 row_mask:0xf bank_mask:0xf\n\t"
                "buffer_load_short_d16 v78, v40, %[rm], 0 offen\n\t"
                "ds_read_b64 v[52:53], v39\n\t"
                "v_add_u32_dpp v40, v54, %[n2] row_newbcast:14 row_mask:0xf bank_mask:0xf\n\t"
                "buffer_load_short_d16_hi v86, v41, %[rm], 0 offen\n\t"
                "v_readlane_b32 s72, %[desc], s75\n\t"
                "s_add_u32 s75, s75, 1\n\t"
                "s_lshr_b32 s73, s72, 16\n\t"
                "s_and_b32 s72, s72, 0xffff\n\t"
                "v_add_u32_dpp v41, v54, %[n2] row_newbcast:15 row_mask:0xf bank_mask:0xf\n\t"
                "buffer_load_short_d16 v79, v40, %[rm], 0 offen\n\t"
                "buffer_load_short_d16_hi v87, v41, %[rm], 0 offen\n\t"
                "s_waitcnt vmcnt(16)\n\t"
                "v_or_b32 v56, v56, v64\n\t"
                "v_or_b32 v57, v57, v65\n\t"
                "v_or_b32 v58, v58, v66\n\t"
                "v_or_b32 v59, v59, v67\n\t"
                "v_or_b32 v60, v60, v68\n\t"
                "v_or_b32 v61, v61, v69\n\t"
                "v_or_b32 v62, v62, v70\n\t"
                "v_or_b32 v63, v63, v71\n\t"
                "s_waitcnt lgkmcnt(2)\n\t"
                "v_mfma_f32_16x16x32_bf16 v[116:119], v[56:59], v[44:47], v[116:119]\n\t"
                "v_mfma_f32_16x16x32_bf16 v[116:119], v[60:63], v[48:51], v[116:119]\n\t"
                "s_sub_u32 s74, s74, 1\n\t"
                "s_cmp_eq_u32 s74, 0\n\t"
                "s_cbranch_scc1 E8_1_%=\n\t"
                "B7_1_%=:\n\t"
                "s_waitcnt lgkmcnt(0)\n\t"
                "v_add_u32_dpp v40, v52, %[n2] row_newbcast:0 row_mask:0xf bank_mask:0xf\n\t"
                "v_add_u32_dpp v41, v52, %[n2] row_newbcast:1 row_mask:0xf bank_mask:0xf\n\t"
                "buffer_load_short_d16 v56, v40, %[rm], 0 offen\n\t"
                "v_and_b32 v43, 0x3c, v55\n\t"
                "v_mad_u32_u24 v43, v43, 20, %[ww]\n\t"
                "v_add_u32_dpp v40, v52, %[n2] row_newbcast:2 row_mask:0xf bank_mask:0xf\n\t"
                "buffer_load_short_d16_hi v64, v41, %[rm], 0 offen\n\t"
                "v_add_u32_dpp v41, v52, %[n2] row_newbcast:3 row_mask:0xf bank_mask:0xf\n\t"
                "buffer_load_short_d16 v57, v40, %[rm], 0 offen\n\t"
                "s_mov_b64 exec, s[76:77]\n\t"
                "ds_write_b16_d16_hi v43, v55\n\t"
                "s_mov_b64 exec, -1\n\t"
                "v_add_u32_dpp v40, v52, %[n2] row_newbcast:4 row_mask:0xf bank_mask:0xf\n\t"
                "buffer_load_short_d16_hi v65, v41, %[rm], 0 offen\n\t"
                "ds_read_b128 v[44:47], %[wr]\n\t"
                "v_add_u32_dpp v41, v52, %[n2] row_newbcast:5 row_mask:0xf bank_mask:0xf\n\t"
                "buffer_load_short_d16 v58, v40, %[rm], 0 offen\n\t"
                "v_add_u32_dpp v40, v52, %[n2] row_newbcast:6 row_mask:0xf bank_mask:0xf\n\t"
                "buffer_load_short_d16_hi v66, v41, %[rm], 0 offen\n\t"
                "s_mov_b64 exec, s[76:77]\n\t"
                "ds_write_b16 v43, v42\n\t"
                "s_mov_b64 exec, s[78:79]\n\t"
                "ds_write_b16_d16_hi v43, v55\n\t"
                "s_mov_b64 exec, -1\n\t"
                "v_add_u32_dpp v41, v52, %[n2] row_newbcast:7 row_mask:0xf bank_mask:0xf\n\t"
                "buffer_load_short_d16 v59, v40, %[rm], 0 offen\n\t"
                "ds_read_b128 v[48:51], %[wr]\n\t"
                "v_add_u32_dpp v40, v52, %[n2] row_newbcast:8 row_mask:0xf bank_mask:0xf\n\t"
                "buffer_load_short_d16_hi v67, v41, %[rm], 0 offen\n\t"
                "v_add_u32_dpp v41, v52, %[n2] row_newbcast:9 row_mask:0xf bank_mask:0xf\n\t"
                "buffer_load_short_d16 v60, v40, %[rm], 0 offen\n\t"
                "s_mov_b64 exec, s[78:79]\n\t"
                "ds_write_b16 v43, v42\n\t"
                "s_mov_b64 exec, -1\n\t"
                "v_add_u32_dpp v40, v52, %[n2] row_newbcast:10 row_mask:0xf bank_mask:0xf\n\t"
                "buffer_load_short_d16_hi v68, v41, %[rm], 0 offen\n\t"
                "v_add_u32 v39, s72, %[kl]\n\t"
                "v_cmp_gt_u32 vcc, s73, v39\n\t"
                "v_add_u32_dpp v41, v52, %[n2] row_newbcast:11 row_mask:0xf bank_mask:0xf\n\t"
                "buffer_load_short_d16 v61, v40, %[rm], 0 offen\n\t"
                "v_add_u32_dpp v40, v52, %[n2] row_newbcast:12 row_mask:0xf bank_mask:0xf\n\t"
                "buffer_load_short_d16_hi v69, v41, %[rm], 0 offen\n\t"
                "v_lshlrev_b32 v39, 3, v39\n\t"
                "s_nop 0\n\t"
                "v_cndmask_b32 v39, 0, v39, vcc\n\t"
                "v_add_u32_dpp v41, v52, %[n2] row_newbcast:13 row_mask:0xf bank_mask:0xf\n\t"
                "buffer_load_short_d16 v62, v40, %[rm], 0 offen\n\t"
                "ds_read_b64 v[54:55], v39\n\t"
                "v_add_u32_dpp v40, v52, %[n2] row_newbcast:14 row_mask:0xf bank_mask:0xf\n\t"
                "buffer_load_short_d16_hi v70, v41, %[rm], 0 offen\n\t"
                "v_readlane_b32 s72, %[desc], s75\n\t"
                "s_add_u32 s75, s75, 1\n\t"
                "s_lshr_b32 s73, s72, 16\n\t"
                "s_and_b32 s72, s72, 0xffff\n\t"
                "v_add_u32_dpp v41, v52, %[n2] row_newbcast:15 row_mask:0xf bank_mask:0xf\n\t"
                "buffer_load_short_d16 v63, v40, %[rm], 0 offen\n\t"
                "buffer_load_short_d16_hi v71, v41, %[rm], 0 offen\n\t"
                "s_waitcnt vmcnt(16)\n\t"
                "v_or_b32 v72, v72, v80\n\t"
                "v_or_b32 v73, v73, v81\n\t"
                "v_or_b32 v74, v74, v82\n\t"
                "v_or_b32 v75, v75, v83\n\t"
                "v_or_b32 v76, v76, v84\n\t"
                "v_or_b32 v77, v77, v85\n\t"
                "v_or_b32 v78, v78, v86\n\t"
                "v_or_b32 v79, v79, v87\n\t"
                "s_waitcnt lgkmcnt(2)\n\t"
                "v_mfma_f32_16x16x32_bf16 v[116:119], v[72:75], v[44:47], v[116:119]\n\t"
                "v_mfma_f32_16x16x32_bf16 v[116:119], v[76:79], v[48:51], v[116:119]\n\t"
                "s_sub_u32 s74, s74, 1\n\t"
                "s_cmp_eq_u32 s74, 0\n\t"
                "s_cbranch_scc0 B7_0_%=\n\t"
                "s_branch E8_0_%=\n\t"
                "E7_1_%=:\n\t"
                "s_setprio 0\n\t"
                "v_readlane_b32 s74, %[cnt], 7\n\t"
                "s_nop 0\n\t"
                "s_cmp_eq_u32 s74, 0\n\t"
                "s_cbranch_scc0 B7_1_%=\n\t"
                "s_branch E8_1_%=\n\t"
                "E8_0_%=:\n\t"
                "s_setprio 0\n\t"
                "v_readlane_b32 s74, %[cnt], 8\n\t"
                "s_nop 0\n\t"
                "s_cmp_eq_u32 s74, 0\n\t"
                "s_cbranch_scc1 E9_0_%=\n\t"
                "B8_0_%=:\n\t"
                "s_waitcnt lgkmcnt(0)\n\t"
                "v_add_u32_dpp v40, v54, %[n2] row_newbcast:0 row_mask:0xf bank_mask:0xf\n\t"
                "v_add_u32_dpp v41, v54, %[n2] row_newbcast:1 row_mask:0xf bank_mask:0xf\n\t"
                "buffer_load_short_d16 v72, v40, %[rm], 0 offen\n\t"
                "v_and_b32 v43, 0x3c, v53\n\t"
                "v_mad_u32_u24 v43, v43, 20, %[ww]\n\t"
                "v_add_u32_dpp v40, v54, %[n2] row_newbcast:2 row_mask:0xf bank_mask:0xf\n\t"
                "buffer_load_short_d16_hi v80, v41, %[rm], 0 offen\n\t"
                "v_add_u32_dpp v41, v54, %[n2] row_newbcast:3 row_mask:0xf bank_mask:0xf\n\t"
                "buffer_load_short_d16 v73, v40, %[rm], 0 offen\n\t"
                "s_mov_b64 exec, s[76:77]\n\t"
                "ds_write_b16_d16_hi v43, v53\n\t"
                "s_mov_b64 exec, -1\n\t"
                "v_add_u32_dpp v40, v54, %[n2] row_newbcast:4 row_mask:0xf bank_mask:0xf\n\t"
                "buffer_load_short_d16_hi v81, v41, %[rm], 0 offen\n\t"
                "ds_read_b128 v[44:47], %[wr]\n\t"
                "v_add_u32_dpp v41, v54, %[n2] row_newbcast:5 row_mask:0xf bank_mask:0xf\n\t"
                "buffer_load_short_d16 v74, v40, %[rm], 0 offen\n\t"
                "v_add_u32_dpp v40, v54, %[n2] row_newbcast:6 row_mask:0xf bank_mask:0xf\n\t"
                "buffer_load_short_d16_hi v82, v41, %[rm], 0 offen\n\t"
                "s_mov_b64 exec, s[76:77]\n\t"
                "ds_write_b16 v43, v42\n\t"
                "s_mov_b64 exec, s[78:79]\n\t"
                "ds_write_b16_d16_hi v43, v53\n\t"
                "s_mov_b64 exec, -1\n\t"
                "v_add_u32_dpp v41, v54, %[n2] row_newbcast:7 row_mask:0xf bank_mask:0xf\n\t"
                "buffer_load_short_d16 v75, v40, %[rm], 0 offen\n\t"
                "ds_read_b128 v[48:51], %[wr]\n\t"
                "v_add_u32_dpp v40, v54, %[n2] row_newbcast:8 row_mask:0xf bank_mask:0xf\n\t"
                "buffer_load_short_d16_hi v83, v41, %[rm], 0 offen\n\t"
                "v_add_u32_dpp v41, v54, %[n2] row_newbcast:9 row_mask:0xf bank_mask:0xf\n\t"
                "buffer_load_short_d16 v76, v40, %[rm], 0 offen\n\t"
                "s_mov_b64 exec, s[78:79]\n\t"
                "ds_write_b16 v43, v42\n\t"
                "s_mov_b64 exec, -1\n\t"
                "v_add_u32_dpp v40, v54, %[n2] row_newbcast:10 row_mask:0xf bank_mask:0xf\n\t"
                "buffer_load_short_d16_hi v84, v41, %[rm], 0 offen\n\t"
                "v_add_u32 v39, s72, %[kl]\n\t"
                "v_cmp_gt_u32 vcc, s73, v39\n\t"
                "v_add_u32_dpp v41, v54, %[n2] row_newbcast:11 row_mask:0xf bank_mask:0xf\n\t"
                "buffer_load_short_d16 v77, v40, %[rm], 0 offen\n\t"
                "v_add_u32_dpp v40, v54, %[n2] row_newbcast:12 row_mask:0xf bank_mask:0xf\n\t"
                "buffer_load_short_d16_hi v85, v41, %[rm], 0 offen\n\t"
                "v_lshlrev_b32 v39, 3, v39\n\t"
                "s_nop 0\n\t"
                "v_cndmask_b32 v39, 0, v39, vcc\n\t"
                "v_add_u32_dpp v41, v54, %[n2] row_newbcast:13 row_mask:0xf bank_mask:0xf\n\t"
                "buffer_load_short_d16 v78, v40, %[rm], 0 offen\n\t"
                "ds_read_b64 v[52:53], v39\n\t"
                "v_add_u32_dpp v40, v54, %[n2] row_newbcast:14 row_mask:0xf bank_mask:0xf\n\t"
                "buffer_load_short_d16_hi v86, v41, %[rm], 0 offen\n\t"
                "v_readlane_b32 s72, %[desc], s75\n\t"
                "s_add_u32 s75, s75, 1\n\t"
                "s_lshr_b32 s73, s72, 16\n\t"
                "s_and_b32 s72, s72, 0xffff\n\t"
                "v_add_u32_dpp v41, v54, %[n2] row_newbcast:15 row_mask:0xf bank_mask:0xf\n\t"
                "buffer_load_short_d16 v79, v40, %[rm], 0 offen\n\t"
                "buffer_load_short_d16_hi v87, v41, %[rm], 0 offen\n\t"
                "s_waitcnt vmcnt(16)\n\t"
                "v_or_b32 v56, v56, v64\n\t"
                "v_or_b32 v57, v57, v65\n\t"
                "v_or_b32 v58, v58, v66\n\t"
                "v_or_b32 v59, v59, v67\n\t"
                "v_or_b32 v60, v60, v68\n\t"
                "v_or_b32 v61, v61, v69\n\t"
                "v_or_b32 v62, v62, v70\n\t"
                "v_or_b32 v63, v63, v71\n\t"
                "s_waitcnt lgkmcnt(2)\n\t"
                "v_mfma_f32_16x16x32_bf16 v[120:123], v[56:59], v[44:47], v[120:123]\n\t"
                "v_mfma_f32_16x16x32_bf16 v[120:123], v[60:63], v[48:51], v[120:123]\n\t"
                "s_sub_u32 s74, s74, 1\n\t"
                "s_cmp_eq_u32 s74, 0\n\t"
                "s_cbranch_scc1 E9_1_%=\n\t"
                "B8_1_%=:\n\t"
                "s_waitcnt lgkmcnt(0)\n\t"
                "v_add_u32_dpp v40, v52, %[n2] row_newbcast:0 row_mask:0xf bank_mask:0xf\n\t"
                "v_add_u32_dpp v41, v52, %[n2] row_newbcast:1 row_mask:0xf bank_mask:0xf\n\t"
                "buffer_load_short_d16 v56, v40, %[rm], 0 offen\n\t"
                "v_and_b32 v43, 0x3c, v55\n\t"
                "v_mad_u32_u24 v43, v43, 20, %[ww]\n\t"
                "v_add_u32_dpp v40, v52, %[n2] row_newbcast:2 row_mask:0xf bank_mask:0xf\n\t"
                "buffer_load_short_d16_hi v64, v41, %[rm], 0 offen\n\t"
                "v_add_u32_dpp v41, v52, %[n2] row_newbcast:3 row_mask:0xf bank_mask:0xf\n\t"
                "buffer_load_short_d16 v57, v40, %[rm], 0 offen\n\t"
                "s_mov_b64 exec, s[76:77]\n\t"
                "ds_write_b16_d16_hi v43, v55\n\t"
                "s_mov_b64 exec, -1\n\t"
                "v_add_u32_dpp v40, v52, %[n2] row_newbcast:4 row_mask:0xf bank_mask:0xf\n\t"
                "buffer_load_short_d16_hi v65, v41, %[rm], 0 offen\n\t"
                "ds_read_b128 v[44:47], %[wr]\n\t"
                "v_add_u32_dpp v41, v52, %[n2] row_newbcast:5 row_mask:0xf bank_mask:0xf\n\t"
                "buffer_load_short_d16 v58, v40, %[rm], 0 offen\n\t"
                "v_add_u32_dpp v40, v52, %[n2] row_newbcast:6 row_mask:0xf bank_mask:0xf\n\t"
                "buffer_load_short_d16_hi v66, v41, %[rm], 0 offen\n\t"
                "s_mov_b64 exec, s[76:77]\n\t"
                "ds_write_b16 v43, v42\n\t"
                "s_mov_b64 exec, s[78:79]\n\t"
                "ds_write_b16_d16_hi v43, v55\n\t"
                "s_mov_b64 exec, -1\n\t"
                "v_add_u32_dpp v41, v52, %[n2] row_newbcast:7 row_mask:0xf bank_mask:0xf\n\t"
                "buffer_load_short_d16 v59, v40, %[rm], 0 offen\n\t"
                "ds_read_b128 v[48:51], %[wr]\n\t"
                "v_add_u32_dpp v40, v52, %[n2] row_newbcast:8 row_mask:0xf bank_mask:0xf\n\t"
                "buffer_load_short_d16_hi v67, v41, %[rm], 0 offen\n\t"
                "v_add_u32_dpp v41, v52, %[n2] row_newbcast:9 row_mask:0xf bank_mask:0xf\n\t"
                "buffer_load_short_d16 v60, v40, %[rm], 0 offen\n\t"
                "s_mov_b64 exec, s[78:79]\n\t"
                "ds_write_b16 v43, v42\n\t"
                "s_mov_b64 exec, -1\n\t"
                "v_add_u32_dpp v40, v52, %[n2] row_newbcast:10 row_mask:0xf bank_mask:0xf\n\t"
                "buffer_load_short_d16_hi v68, v41, %[rm], 0 offen\n\t"
                "v_add_u32 v39, s72, %[kl]\n\t"
                "v_cmp_gt_u32 vcc, s73, v39\n\t"
                "v_add_u32_dpp v41, v52, %[n2] row_newbcast:11 row_mask:0xf bank_mask:0xf\n\t"
                "buffer_load_short_d16 v61, v40, %[rm], 0 offen\n\t"
                "v_add_u32_dpp v40, v52, %[n2] row_newbcast:12 row_mask:0xf bank_mask:0xf\n\t"
                "buffer_load_short_d16_hi v69, v41, %[rm], 0 offen\n\t"
                "v_lshlrev_b32 v39, 3, v39\n\t"
                "s_nop 0\n\t"
                "v_cndmask_b32 v39, 0, v39, vcc\n\t"
                "v_add_u32_dpp v41, v52, %[n2] row_newbcast:13 row_mask:0xf bank_mask:0xf\n\t"
                "buffer_load_short_d16 v62, v40, %[rm], 0 offen\n\t"
                "ds_read_b64 v[54:55], v39\n\t"
                "v_add_u32_dpp v40, v52, %[n2] row_newbcast:14 row_mask:0xf bank_mask:0xf\n\t"
                "buffer_load_short_d16_hi v70, v41, %[rm], 0 offen\n\t"
                "v_readlane_b32 s72, %[desc], s75\n\t"
                "s_add_u32 s75, s75, 1\n\t"
                "s_lshr_b32 s73, s72, 16\n\t"
                "s_and_b32 s72, s72, 0xffff\n\t"
                "v_add_u32_dpp v41, v52, %[n2] row_newbcast:15 row_mask:0xf bank_mask:0xf\n\t"
                "buffer_load_short_d16 v63, v40, %[rm], 0 offen\n\t"
                "buffer_load_short_d16_hi v71, v41, %[rm], 0 offen\n\t"
                "s_waitcnt vmcnt(16)\n\t"
                "v_or_b32 v72, v72, v80\n\t"
                "v_or_b32 v73, v73, v81\n\t"
                "v_or_b32 v74, v74, v82\n\t"
                "v_or_b32 v75, v75, v83\n\t"
                "v_or_b32 v76, v76, v84\n\t"
                "v_or_b32 v77, v77, v85\n\t"
                "v_or_b32 v78, v78, v86\n\t"
                "v_or_b32 v79, v79, v87\n\t"
                "s_waitcnt lgkmcnt(2)\n\t"
                "v_mfma_f32_16x16x32_bf16 v[120:123], v[72:75], v[44:47], v[120:123]\n\t"
                "v_mfma_f32_16x16x32_bf16 v[120:123], v[76:79], v[48:51], v[120:123]\n\t"
                "s_sub_u32 s74, s74, 1\n\t"
                "s_cmp_eq_u32 s74, 0\n\t"
                "s_cbranch_scc0 B8_0_%=\n\t"
                "s_branch E9_0_%=\n\t"
                "E8_1_%=:\n\t"
                "s_setprio 0\n\t"
                "v_readlane_b32 s74, %[cnt], 8\n\t"
                "s_nop 0\n\t"
                "s_cmp_eq_u32 s74, 0\n\t"
                "s_cbranch_scc0 B8_1_%=\n\t"
                "s_branch E9_1_%=\n\t"
                "E9_0_%=:\n\t"
                "s_setprio 0\n\t"
                "v_readlane_b32 s74, %[cnt], 9\n\t"
                "s_nop 0\n\t"
                "s_cmp_eq_u32 s74, 0\n\t"
                "s_cbranch_scc1 E10_0_%=\n\t"
                "B9_0_%=:\n\t"
                "s_waitcnt lgkmcnt(0)\n\t"
                "v_add_u32_dpp v40, v54, %[n2] row_newbcast:0 row_mask:0xf bank_mask:0xf\n\t"
                "v_add_u32_dpp v41, v54, %[n2] row_newbcast:1 row_mask:0xf bank_mask:0xf\n\t"
                "buffer_load_short_d16 v72, v40, %[rm], 0 offen\n\t"
                "v_and_b32 v43, 0x3c, v53\n\t"
                "v_mad_u32_u24 v43, v43, 20, %[ww]\n\t"
                "v_add_u32_dpp v40, v54, %[n2] row_newbcast:2 row_mask:0xf bank_mask:0xf\n\t"
                "buffer_load_short_d16_hi v80, v41, %[rm], 0 offen\n\t"
                "v_add_u32_dpp v41, v54, %[n2] row_newbcast:3 row_mask:0xf bank_mask:0xf\n\t"
                "buffer_load_short_d16 v73, v40, %[rm], 0 offen\n\t"
                "s_mov_b64 exec, s[76:77]\n\t"
                "ds_write_b16_d16_hi v43, v53\n\t"
                "s_mov_b64 exec, -1\n\t"
                "v_add_u32_dpp v40, v54, %[n2] row_newbcast:4 row_mask:0xf bank_mask:0xf\n\t"
                "buffer_load_short_d16_hi v81, v41, %[rm], 0 offen\n\t"
                "ds_read_b128 v[44:47], %[wr]\n\t"
                "v_add_u32_dpp v41, v54, %[n2] row_newbcast:5 row_mask:0xf bank_mask:0xf\n\t"
                "buffer_load_short_d16 v74, v40, %[rm], 0 offen\n\t"
                "v_add_u32_dpp v40, v54, %[n2] row_newbcast:6 row_mask:0xf bank_mask:0xf\n\t"
                "buffer_load_short_d16_hi v82, v41, %[rm], 0 offen\n\t"
                "s_mov_b64 exec, s[76:77]\n\t"
                "ds_write_b16 v43, v42\n\t"
                "s_mov_b64 exec, s[78:79]\n\t"
                "ds_write_b16_d16_hi v43, v53\n\t"
                "s_mov_b64 exec, -1\n\t"
                "v_add_u32_dpp v41, v54, %[n2] row_newbcast:7 row_mask:0xf bank_mask:0xf\n\t"
                "buffer_load_short_d16 v75, v40, %[rm], 0 offen\n\t"
                "ds_read_b128 v[48:51], %[wr]\n\t"
                "v_add_u32_dpp v40, v54, %[n2] row_newbcast:8 row_mask:0xf bank_mask:0xf\n\t"
                "buffer_load_short_d16_hi v83, v41, %[rm], 0 offen\n\t"
                "v_add_u32_dpp v41, v54, %[n2] row_newbcast:9 row_mask:0xf bank_mask:0xf\n\t"
                "buffer_load_short_d16 v76, v40, %[rm], 0 offen\n\t"
                "s_mov_b64 exec, s[78:79]\n\t"
                "ds_write_b16 v43, v42\n\t"
                "s_mov_b64 exec, -1\n\t"
                "v_add_u32_dpp v40, v54, %[n2] row_newbcast:10 row_mask:0xf bank_mask:0xf\n\t"
                "buffer_load_short_d16_hi v84, v41, %[rm], 0 offen\n\t"
                "v_add_u32 v39, s72, %[kl]\n\t"
                "v_cmp_gt_u32 vcc, s73, v39\n\t"
                "v_add_u32_dpp v41, v54, %[n2] row_newbcast:11 row_mask:0xf bank_mask:0xf\n\t"
                "buffer_load_short_d16 v77, v40, %[rm], 0 offen\n\t"
                "v_add_u32_dpp v40, v54, %[n2] row_newbcast:12 row_mask:0xf bank_mask:0xf\n\t"
                "buffer_load_short_d16_hi v85, v41, %[rm], 0 offen\n\t"
                "v_lshlrev_b32 v39, 3, v39\n\t"
                "s_nop 0\n\t"
                "v_cndmask_b32 v39, 0, v39, vcc\n\t"
                "v_add_u32_dpp v41, v54, %[n2] row_newbcast:13 row_mask:0xf bank_mask:0xf\n\t"
                "buffer_load_short_d16 v78, v40, %[rm], 0 offen\n\t"
                "ds_read_b64 v[52:53], v39\n\t"
                "v_add_u32_dpp v40, v54, %[n2] row_newbcast:14 row_mask:0xf bank_mask:0xf\n\t"
                "buffer_load_short_d16_hi v86, v41, %[rm], 0 offen\n\t"
                "v_readlane_b32 s72, %[desc], s75\n\t"
                "s_add_u32 s75, s75, 1\n\t"
                "s_lshr_b32 s73, s72, 16\n\t"
                "s_and_b32 s72, s72, 0xffff\n\t"
                "v_add_u32_dpp v41, v54, %[n2] row_newbcast:15 row_mask:0xf bank_mask:0xf\n\t"
                "buffer_load_short_d16 v79, v40, %[rm], 0 offen\n\t"
                "buffer_load_short_d16_hi v87, v41, %[rm], 0 offen\n\t"
                "s_waitcnt vmcnt(16)\n\t"
                "v_or_b32 v56, v56, v64\n\t"
                "v_or_b32 v57, v57, v65\n\t"
                "v_or_b32 v58, v58, v66\n\t"
                "v_or_b32 v59, v59, v67\n\t"
                "v_or_b32 v60, v60, v68\n\t"
                "v_or_b32 v61, v61, v69\n\t"
                "v_or_b32 v62, v62, v70\n\t"
                "v_or_b32 v63, v63, v71\n\t"
                "s_waitcnt lgkmcnt(2)\n\t"
                "v_mfma_f32_16x16x32_bf16 v[124:127], v[56:59], v[44:47], v[124:127]\n\t"
                "v_mfma_f32_16x16x32_bf16 v[124:127], v[60:63], v[48:51], v[124:127]\n\t"
                "s_sub_u32 s74, s74, 1\n\t"
                "s_cmp_eq_u32 s74, 0\n\t"
                "s_cbranch_scc1 E10_1_%=\n\t"
                "B9_1_%=:\n\t"
                "s_waitcnt lgkmcnt(0)\n\t"
                "v_add_u32_dpp v40, v52, %[n2] row_newbcast:0 row_mask:0xf bank_mask:0xf\n\t"
                "v_add_u32_dpp v41, v52, %[n2] row_newbcast:1 row_mask:0xf bank_mask:0xf\n\t"
                "buffer_load_short_d16 v56, v40, %[rm], 0 offen\n\t"
                "v_and_b32 v43, 0x3c, v55\n\t"
                "v_mad_u32_u24 v43, v43, 20, %[ww]\n\t"
                "v_add_u32_dpp v40, v52, %[n2] row_newbcast:2 row_mask:0xf bank_mask:0xf\n\t"
                "buffer_load_short_d16_hi v64, v41, %[rm], 0 offen\n\t"
                "v_add_u32_dpp v41, v52, %[n2] row_newbcast:3 row_mask:0xf bank_mask:0xf\n\t"
                "buffer_load_short_d16 v57, v40, %[rm], 0 offen\n\t"
                "s_mov_b64 exec, s[76:77]\n\t"
                "ds_write_b16_d16_hi v43, v55\n\t"
                "s_mov_b64 exec, -1\n\t"
                "v_add_u32_dpp v40, v52, %[n2] row_newbcast:4 row_mask:0xf bank_mask:0xf\n\t"
                "buffer_load_short_d16_hi v65, v41, %[rm], 0 offen\n\t"
                "ds_read_b128 v[44:47], %[wr]\n\t"
                "v_add_u32_dpp v41, v52, %[n2] row_newbcast:5 row_mask:0xf bank_mask:0xf\n\t"
                "buffer_load_short_d16 v58, v40, %[rm], 0 offen\n\t"
                "v_add_u32_dpp v40, v52, %[n2] row_newbcast:6 row_mask:0xf bank_mask:0xf\n\t"
                "buffer_load_short_d16_hi v66, v41, %[rm], 0 offen\n\t"
                "s_mov_b64 exec, s[76:77]\n\t"
                "ds_write_b16 v43, v42\n\t"
                "s_mov_b64 exec, s[78:79]\n\t"
                "ds_write_b16_d16_hi v43, v55\n\t"
                "s_mov_b64 exec, -1\n\t"
                "v_add_u32_dpp v41, v52, %[n2] row_newbcast:7 row_mask:0xf bank_mask:0xf\n\t"
                "buffer_load_short_d16 v59, v40, %[rm], 0 offen\n\t"
                "ds_read_b128 v[48:51], %[wr]\n\t"
                "v_add_u32_dpp v40, v52, %[n2] row_newbcast:8 row_mask:0xf bank_mask:0xf\n\t"
                "buffer_load_short_d16_hi v67, v41, %[rm], 0 offen\n\t"
                "v_add_u32_dpp v41, v52, %[n2] row_newbcast:9 row_mask:0xf bank_mask:0xf\n\t"
                "buffer_load_short_d16 v60, v40, %[rm], 0 offen\n\t"
                "s_mov_b64 exec, s[78:79]\n\t"
                "ds_write_b16 v43, v42\n\t"
                "s_mov_b64 exec, -1\n\t"
                "v_add_u32_dpp v40, v52, %[n2] row_newbcast:10 row_mask:0xf bank_mask:0xf\n\t"
                "buffer_load_short_d16_hi v68, v41, %[rm], 0 offen\n\t"
                "v_add_u32 v39, s72, %[kl]\n\t"
                "v_cmp_gt_u32 vcc, s73, v39\n\t"
                "v_add_u32_dpp v41, v52, %[n2] row_newbcast:11 row_mask:0xf bank_mask:0xf\n\t"
                "buffer_load_short_d16 v61, v40, %[rm], 0 offen\n\t"
                "v_add_u32_dpp v40, v52, %[n2] row_newbcast:12 row_mask:0xf bank_mask:0xf\n\t"
                "buffer_load_short_d16_hi v69, v41, %[rm], 0 offen\n\t"
                "v_lshlrev_b32 v39, 3, v39\n\t"
                "s_nop 0\n\t"
                "v_cndmask_b32 v39, 0, v39, vcc\n\t"
                "v_add_u32_dpp v41, v52, %[n2] row_newbcast:13 row_mask:0xf bank_mask:0xf\n\t"
                "buffer_load_short_d16 v62, v40, %[rm], 0 offen\n\t"
                "ds_read_b64 v[54:55], v39\n\t"
                "v_add_u32_dpp v40, v52, %[n2] row_newbcast:14 row_mask:0xf bank_mask:0xf\n\t"
                "buffer_load_short_d16_hi v70, v41, %[rm], 0 offen\n\t"
                "v_readlane_b32 s72, %[desc], s75\n\t"
                "s_add_u32 s75, s75, 1\n\t"
                "s_lshr_b32 s73, s72, 16\n\t"
                "s_and_b32 s72, s72, 0xffff\n\t"
                "v_add_u32_dpp v41, v52, %[n2] row_newbcast:15 row_mask:0xf bank_mask:0xf\n\t"
                "buffer_load_short_d16 v63, v40, %[rm], 0 offen\n\t"
                "buffer_load_short_d16_hi v71, v41, %[rm], 0 offen\n\t"
                "s_waitcnt vmcnt(16)\n\t"
                "v_or_b32 v72, v72, v80\n\t"
                "v_or_b32 v73, v73, v81\n\t"
                "v_or_b32 v74, v74, v82\n\t"
                "v_or_b32 v75, v75, v83\n\t"
                "v_or_b32 v76, v76, v84\n\t"
                "v_or_b32 v77, v77, v85\n\t"
                "v_or_b32 v78, v78, v86\n\t"
                "v_or_b32 v79, v79, v87\n\t"
                "s_waitcnt lgkmcnt(2)\n\t"
                "v_mfma_f32_16x16x32_bf16 v[124:127], v[72:75], v[44:47], v[124:127]\n\t"
                "v_mfma_f32_16x16x32_bf16 v[124:127], v[76:79], v[48:51], v[124:127]\n\t"
                "s_sub_u32 s74, s74, 1\n\t"
                "s_cmp_eq_u32 s74, 0\n\t"
                "s_cbranch_scc0 B9_0_%=\n\t"
                "s_branch E10_0_%=\n\t"
                "E9_1_%=:\n\t"
                "s_setprio 0\n\t"
                "v_readlane_b32 s74, %[cnt], 9\n\t"
                "s_nop 0\n\t"
                "s_cmp_eq_u32 s74, 0\n\t"
                "s_cbranch_scc0 B9_1_%=\n\t"
                "s_branch E10_1_%=\n\t"
                "E10_0_%=:\n\t"
                "E10_1_%=:\n\t"
                "s_waitcnt vmcnt(0) lgkmcnt(0)\n\t"
                "s_nop 7"
                    : "+{v[88:91]}"(acc0), "+{v[92:95]}"(acc1), "+{v[96:99]}"(acc2), "+{v[100:103]}"(acc3), "+{v[104:107]}"(acc4), "+{v[108:111]}"(acc5), "+{v[112:115]}"(acc6), "+{v[116:119]}"(acc7), "+{v[120:123]}"(acc8), "+{v[124:127]}"(acc9)
                    : [desc] "v"(desc), [cnt] "v"(cntv), [kl] "v"(kl), [n2] "v"(n2), [ww] "v"(ww), [wr] "v"(wr), [rm] "s"(rm)
                    : "vcc", "scc", "memory", "v38", "v39", "v40", "v41", "v42", "v43", "v44", "v45", "v46", "v47", "v48", "v49", "v50", "v51", "v52", "v53", "v54", "v55", "v56", "v57", "v58", "v59", "v60", "v61", "v62", "v63", "v64", "v65", "v66", "v67", "v68", "v69", "v70", "v71", "v72", "v73", "v74", "v75", "v76", "v77", "v78", "v79", "v80", "v81", "v82", "v83", "v84", "v85", "v86", "v87", "s72", "s73", "s74", "s75", "s76", "s77", "s78", "s79", "s80", "s81", "s82", "s83", "s84", "s85", "s86", "s87", "s88", "s89", "s90", "s91");
            }
            lds_signal(lds, P2_SYNC + 8 + par * 4, lane);
            lo += CAP;
            it_norm(p, lo);
        }
        __builtin_amdgcn_s_setprio(0);

        u32x2 mv[TPC]; u32 dv[TPC];
        const auto rmm = __builtin_amdgcn_make_buffer_rsrc((void*)(m + (size_t)ob * EPB * 16), 0, EPB * 32, 0x00020000);
        const auto rd = __builtin_amdgcn_make_buffer_rsrc((void*)(dist + (size_t)ob * EPB), 0, EPB * 2, 0x00020000);
        const auto ro = __builtin_amdgcn_make_buffer_rsrc((void*)(out + (size_t)ob * EPB * 16), 0, EPB * 32, 0x00020000);
#pragma unroll
        for (int k = 0; k < TPC; k++) {
            const u32 el = (wave * TPC + k) * 16 + i16;
            dv[k] = __builtin_amdgcn_raw_buffer_load_b16(rd, el * 2, 0, 0);
        }
        SCHED_FENCE();
#pragma unroll
        for (int k = 0; k < TPC; k++) {
            const u32 el = (wave * TPC + k) * 16 + i16;
            mv[k] = __builtin_amdgcn_raw_buffer_load_b64(rmm, el * 32 + q * 8, 0, 0);
        }
        SCHED_FENCE();
        const float ce0 = __uint_as_float(cew << 16), ce1 = __uint_as_float(cew & 0xffff0000u);
        const bf16x8_t Awd = __builtin_bit_cast(bf16x8_t, u32x4{wd0, wd1, wd0, wd1});
        const bf16x8_t Awu = __builtin_bit_cast(bf16x8_t, u32x4{wu0, wu1, wu0, wu1});
        const bf16x8_t Aws = __builtin_bit_cast(bf16x8_t, u32x4{ws0, 0u, 0u, 0u});
        const bf16x8_t Awr = __builtin_bit_cast(bf16x8_t, u32x4{wr0, 0u, 0u, 0u});
        auto split = [&](const f32x4& v) {
            const u32 h0 = pkbf(v[0], v[1]), h1 = pkbf(v[2], v[3]);
            const u32 l0 = pkbf(v[0] - bf2f(h0 & 0xffffu), v[1] - __uint_as_float(h0 & 0xffff0000u));
            const u32 l1 = pkbf(v[2] - bf2f(h1 & 0xffffu), v[3] - __uint_as_float(h1 & 0xffff0000u));
            return __builtin_bit_cast(bf16x8_t, u32x4{h0, h1, l0, l1});
        };
#pragma unroll
        for (int k = 0; k < TPC; k++) {
            const f32x4 sv = k == 0 ? acc0 : k == 1 ? acc1 : k == 2 ? acc2 : k == 3 ? acc3 : k == 4 ? acc4 : k == 5 ? acc5 : k == 6 ? acc6 : k == 7 ? acc7 : k == 8 ? acc8 : acc9;
            const u32 el = (wave * TPC + k) * 16 + i16;
            const float d = bf2f(dv[k]);
            u32 xp = pkbf(d - ce0, d - ce1);
            float x0 = __uint_as_float(xp << 16), x1 = __uint_as_float(xp & 0xffff0000u);
            xp = pkbf(x0 * x0, x1 * x1);
            xp = pkbf(-10.0f * __uint_as_float(xp << 16), -10.0f * __uint_as_float(xp & 0xffff0000u));
            x0 = __uint_as_float(xp << 16); x1 = __uint_as_float(xp & 0xffff0000u);
            const u32 rb = pkbf(__builtin_amdgcn_exp2f(x0 * 1.4426950408889634f), __builtin_amdgcn_exp2f(x1 * 1.4426950408889634f));
            const bf16x8_t Br = __builtin_bit_cast(bf16x8_t, u32x4{rb, 0u, 0u, 0u});
            const f32x4 z = {0.f, 0.f, 0.f, 0.f};
            const f32x4 md = __builtin_amdgcn_mfma_f32_16x16x32_bf16(Awd, split(sv), z, 0, 0, 0);
            const f32x4 g = __builtin_amdgcn_mfma_f32_16x16x32_bf16(Aws, Br, z, 0, 0, 0);
            const f32x4 hh = __builtin_amdgcn_mfma_f32_16x16x32_bf16(Awr, Br, z, 0, 0, 0);
            const f32x4 agg = md * g;
            const bf16x8_t Bagg = __builtin_bit_cast(bf16x8_t, u32x4{pkbf(agg[0], agg[1]), pkbf(agg[2], agg[3]), 0u, 0u});
            const f32x4 up = __builtin_amdgcn_mfma_f32_16x16x32_bf16(Awu, Bagg, z, 0, 0, 0);
            const u32 m0 = mv[k][0], m1 = mv[k][1];
            const float o0 = bf2f(m0 & 0xffffu) + up[0] * hh[0];
            const float o1 = __uint_as_float(m0 & 0xffff0000u) + up[1] * hh[1];
            const float o2 = bf2f(m1 & 0xffffu) + up[2] * hh[2];
            const float o3 = __uint_as_float(m1 & 0xffff0000u) + up[3] * hh[3];
            __builtin_amdgcn_raw_buffer_store_b64(u32x2{pkbf(o0, o1), pkbf(o2, o3)}, ro, el * 32 + q * 8, 0, 0);
        }
    }
}

__global__ void gen_zero(float4* S, int n4) {
    const int i = blockIdx.x * blockDim.x + threadIdx.x;
    if (i < n4) S[i] = make_float4(0.f, 0.f, 0.f, 0.f);
}
__global__ void gen_scatter(const u16* __restrict__ m, const int* __restrict__ idx_in, const int* __restrict__ idx_out,
                            const u16* __restrict__ angle, const u16* __restrict__ ang_freq, float* S, int T) {
    const long long g = (long long)blockIdx.x * blockDim.x + threadIdx.x;
    const int t = (int)(g >> 4), f = (int)(g & 15);
    if (t >= T) return;
    const float c = rbf16(cos_any(rbf16(bf2f(ang_freq[0]) * bf2f(angle[t]))));
    atomicAdd(&S[(size_t)idx_out[t] * 16 + f], bf2f(m[(size_t)idx_in[t] * 16 + f]) * c);
}
__global__ void __launch_bounds__(256) gen_epilogue(const u16* __restrict__ m, const u16* __restrict__ dist, const u16* __restrict__ centers,
                                                    const u16* __restrict__ w_rbf, const u16* __restrict__ w_sbf, const u16* __restrict__ w_down,
                                                    const u16* __restrict__ w_up, const float* __restrict__ S, u16* __restrict__ out, int E) {
    const int e = blockIdx.x * blockDim.x + threadIdx.x;
    if (e >= E) return;
    float rbf[8], agg[16];
    const float d = bf2f(dist[e]);
#pragma unroll
    for (int r = 0; r < 8; r++) {
        float x = rbf16(d - bf2f(centers[r]));
        x = rbf16(-10.0f * rbf16(x * x));
        rbf[r] = rbf16(expf(x));
    }
#pragma unroll
    for (int j = 0; j < 16; j++) {
        float a = 0.f, g = 0.f;
#pragma unroll
        for (int k = 0; k < 16; k++) a += S[(size_t)e * 16 + k] * bf2f(w_down[k * 16 + j]);
#pragma unroll
        for (int r = 0; r < 8; r++) g += rbf[r] * bf2f(w_sbf[r * 16 + j]);
        agg[j] = a * g;
    }
#pragma unroll
    for (int j = 0; j < 16; j++) {
        float a = 0.f, g = 0.f;
#pragma unroll
        for (int k = 0; k < 16; k++) a += agg[k] * bf2f(w_up[k * 16 + j]);
#pragma unroll
        for (int r = 0; r < 8; r++) g += rbf[r] * bf2f(w_rbf[r * 16 + j]);
        out[(size_t)e * 16 + j] = (u16)pkbf(bf2f(m[(size_t)e * 16 + j]) + a * g, 0.f);
    }
}

}

extern "C" void kernel_launch(void* const* d_in, const int* in_sizes, int n_in,
                              void* d_out, int out_size, void* d_ws, size_t ws_size,
                              hipStream_t stream) {
    const u16* m = (const u16*)d_in[0];
    const int* idx_in = (const int*)d_in[1];
    const int* idx_out = (const int*)d_in[2];
    const u16* dist = (const u16*)d_in[3];
    const u16* angle = (const u16*)d_in[4];
    const u16* centers = (const u16*)d_in[5];
    const u16* ang_freq = (const u16*)d_in[6];
    const u16* w_rbf = (const u16*)d_in[7];
    const u16* w_sbf = (const u16*)d_in[8];
    const u16* w_down = (const u16*)d_in[9];
    const u16* w_up = (const u16*)d_in[10];
    const int E = in_sizes[3], T = in_sizes[1];
    if (E != E_ || T != T_ || ws_size < (size_t)NCH * CSTR * 6 + (size_t)NCH * NOB * (16 + 4 * NPH) + 256) {
        float* S = (float*)d_ws;
        gen_zero<<<(E * 4 + 255) / 256, 256, 0, stream>>>((float4*)S, E * 4);
        gen_scatter<<<(int)(((long long)T * 16 + 255) / 256), 256, 0, stream>>>(m, idx_in, idx_out, angle, ang_freq, S, T);
        gen_epilogue<<<(E + 255) / 256, 256, 0, stream>>>(m, dist, centers, w_rbf, w_sbf, w_down, w_up, S, (u16*)d_out, E);
        return;
    }
    char* ws = (char*)d_ws;
    char* rec = ws;
    u32x4* tab = (u32x4*)(ws + (((size_t)NCH * CSTR * 6 + 255) / 256) * 256);
    u32* side = (u32*)(tab + (size_t)NCH * NOB);
    p1_bin<<<NWG1, 1024, 0, stream>>>(idx_in, idx_out, angle, ang_freq, rec, tab, side);
    p2_acc<<<NOB, 1024, 0, stream>>>(m, dist, centers, w_rbf, w_sbf, w_down, w_up, rec, tab, side, (u16*)d_out);
}
```
